```python
import torch

def _softmax(x, axis=-1):
    return torch.softmax(x, dim=axis)

def _gelu(x):
    return torch.nn.functional.gelu(x, approximate='tanh')

def _silu(x):
    return torch.nn.functional.silu(x)

def _sigmoid(x):
    return torch.sigmoid(x)

def _layernorm(x, axis=-1, eps=1e-05):
    m = x.mean(dim=axis, keepdim=True)
    v = x.var(dim=axis, keepdim=True, unbiased=False)
    return (x - m) / torch.sqrt(v + eps)

def _scatter_add(tgt, idx, src, axis=0):
    return tgt.clone().index_add_(axis, idx, src)

def _segment_sum(src, seg, n):
    out = torch.zeros((n,) + src.shape[1:], dtype=src.dtype)
    return out.index_add_(0, seg, src)

def _pad(x, pw):
    flat = tuple((v for pair in reversed(pw) for v in pair))
    return torch.nn.functional.pad(x, flat)
kernel_name = 'gated_mix__grouped_kv_mqa__mixer_gmlp__prompt'

def setup_inputs():

    _w = lambda a: (lambda t: t.to(torch.bfloat16) if t.is_floating_point() else t)(torch.as_tensor(a))

    def _A_setup():
        B, S, D, H = (1, 2048, 640, 32)
        Dh = D // H
        return {'x': _w((lambda *s: torch.randn(*s, dtype=torch.bfloat16) * 0.05)(B, S, D)), 'wq': _w((lambda *s: torch.randn(*s, dtype=torch.bfloat16) * 1.6)(D, H * Dh)), 'wk': _w((lambda *s: torch.randn(*s, dtype=torch.bfloat16) * 1.6)(D, Dh)), 'wv': _w((lambda *s: torch.randn(*s, dtype=torch.bfloat16) * 0.2)(D, Dh)), 'wo': _w((lambda *s: torch.randn(*s, dtype=torch.bfloat16) * 0.2)(H * Dh, D))}
    _A_in = _A_setup()

    def _B_setup():
        B, S, D, F = (1, 2048, 640, 1728)
        Fh = F // 2
        return {'x': _w((lambda *s: torch.randn(*s, dtype=torch.bfloat16) * 0.05)(B, S, D)), 'w_in': _w((lambda *s: torch.randn(*s, dtype=torch.bfloat16) * 0.8)(D, 2 * Fh)), 'w_sp': _w(0.01 * (lambda *s: torch.randn(*s, dtype=torch.bfloat16) * 0.8)(S, S)), 'b_sp': _w(1.0 + 0.01 * (lambda *s: torch.randn(*s, dtype=torch.bfloat16) * 0.05)(S)), 'w_out': _w((lambda *s: torch.randn(*s, dtype=torch.bfloat16) * 0.05)(Fh, D))}
    _B_in = _B_setup()
    return {'A_x': _A_in['x'], 'A_wq': _A_in['wq'], 'A_wk': _A_in['wk'], 'A_wv': _A_in['wv'], 'A_wo': _A_in['wo'], 'B_x': _B_in['x'], 'B_w_in': _B_in['w_in'], 'B_w_sp': _B_in['w_sp'], 'B_b_sp': _B_in['b_sp'], 'B_w_out': _B_in['w_out'], 'gate_w': _w((lambda *s: torch.randn(*s, dtype=torch.bfloat16) * 0.05)(640) * 16.0)}

def reference(A_x, A_wq, A_wk, A_wv, A_wo, B_x, B_w_in, B_w_sp, B_b_sp, B_w_out, gate_w):

    def _A_ref(x, wq, wk, wv, wo):
        B, S, D = x.shape
        H = 32
        Dh = D // H
        q = (lambda x, *d: x.permute(*d))((x @ wq).reshape(B, S, H, Dh), 0, 2, 1, 3)
        k = x @ wk
        v = x @ wv
        scores = torch.einsum('bhqd,bkd->bhqk', q, k) * Dh ** (-0.5)
        p = _softmax(scores, axis=-1)
        o = torch.einsum('bhqk,bkd->bhqd', p, v)
        return (lambda x, *d: x.permute(*d))(o, 0, 2, 1, 3).reshape(B, S, H * Dh) @ wo

    def _B_ref(x, w_in, w_sp, b_sp, w_out):
        B, S, D = x.shape
        F = w_in.shape[-1]
        Fh = F // 2
        z = _gelu(torch.einsum('bsd,df->bsf', x, w_in))
        z1 = z[..., :Fh].clone()
        z2 = _layernorm(z[..., Fh:], axis=-1)
        g = torch.einsum('ts,bsf->btf', w_sp, z2) + b_sp[None, :, None]
        return torch.einsum('bsf,fd->bsd', z1 * g, w_out)
    x = A_x
    g = _sigmoid((x * gate_w).sum(-1, keepdims=True))
    a = _A_ref(x, A_wq, A_wk, A_wv, A_wo)
    b = _B_ref(x, B_w_in, B_w_sp, B_b_sp, B_w_out)
    return g * a + (1 - g) * b
```

```cpp
#include <hip/hip_runtime.h>
#include <hip/hip_bf16.h>
#include <stdint.h>

typedef short  s16x8  __attribute__((ext_vector_type(8)));
typedef short  s16x4  __attribute__((ext_vector_type(4)));
typedef __bf16 bh8    __attribute__((ext_vector_type(8)));
typedef __bf16 bh2    __attribute__((ext_vector_type(2)));
typedef float  f32x16 __attribute__((ext_vector_type(16)));
typedef float  f32x2  __attribute__((ext_vector_type(2)));
typedef unsigned u32x4 __attribute__((ext_vector_type(4)));
typedef unsigned u32x2 __attribute__((ext_vector_type(2)));
typedef unsigned short u16;
typedef __attribute__((address_space(3))) char* ldsp;
typedef __attribute__((address_space(1))) unsigned gu32;

#define DI __device__ __forceinline__
#define SBAR() __builtin_amdgcn_sched_barrier(0)
#define PIN(x) asm volatile("" : "+v"(x))

constexpr int S = 2048, D = 640, FH = 864, F2 = 1728, ZW = 896;
constexpr int KVT = 2560;
constexpr size_t WS_FLAG = 0;
constexpr size_t WS_GATE = 4096;
constexpr size_t WS_KV   = WS_GATE + 8192;
constexpr size_t WS_Q    = WS_KV + 64 * KVT;
constexpr size_t WS_O    = WS_Q + (size_t)S * D * 2;
constexpr size_t WS_Z2R  = WS_O + (size_t)S * D * 2;
constexpr size_t WS_Z2N  = WS_Z2R + (size_t)S * ZW * 2;
constexpr size_t WS_U    = WS_Z2N + (size_t)S * ZW * 2;

DI f32x16 mfma16(s16x8 a, s16x8 b, f32x16 c) {
    return __builtin_amdgcn_mfma_f32_32x32x16_bf16(__builtin_bit_cast(bh8, a), __builtin_bit_cast(bh8, b), c, 0, 0, 0);
}
DI f32x16 mfma8(u32x2 a, u32x2 b, f32x16 c) {
    return __builtin_amdgcn_mfma_f32_32x32x8bf16_1k(__builtin_bit_cast(s16x4, a), __builtin_bit_cast(s16x4, b), c, 0, 0, 0);
}
DI unsigned pk2(float lo, float hi) { f32x2 p = {lo, hi}; return __builtin_bit_cast(unsigned, __builtin_convertvector(p, bh2)); }
DI float bflo(unsigned u) { return __uint_as_float(u << 16); }
DI float bfhi(unsigned u) { return __uint_as_float(u & 0xffff0000u); }
DI float gelu_t(float x) {
    const float k0 = -2.302208198f;
    const float k1 = k0 * 0.044715f;
    float u = x * __builtin_fmaf(x * x, k1, k0);
    return x * __builtin_amdgcn_rcpf(1.0f + __builtin_amdgcn_exp2f(u));
}
DI void glds16(const void* sbase, unsigned voff, unsigned lds) {
    asm volatile("s_mov_b32 m0, %2\n\ts_nop 0\n\tglobal_load_lds_dwordx4 %0, %1" :: "v"(voff), "s"(sbase), "s"(lds) : "memory");
}
#define WAIT_BAR(N) asm volatile("s_waitcnt vmcnt(" #N ") lgkmcnt(0)\n\ts_barrier" ::: "memory")
DI s16x8 ld128(ldsp p) { return *(const __attribute__((address_space(3))) s16x8*)p; }
DI s16x4 ldtr(ldsp p) { return __builtin_amdgcn_ds_read_tr16_b64_v4i16((__attribute__((address_space(3))) s16x4*)p); }
DI s16x8 cat(s16x4 a, s16x4 b) { return __builtin_shufflevector(a, b, 0, 1, 2, 3, 4, 5, 6, 7); }

template <int BT, int XMODE>
DI void gemm_core(f32x16 (&acc)[BT / 64][2], const char* X, int ldx, const char* W, int ldw, int nk16, ldsp lds, int wave, int lane) {
    constexpr int NTT = BT / 64, XS = BT * 128, WSZ = 16384, XP = BT / 32;
    const unsigned ldsX = (unsigned)(uintptr_t)lds, ldsW = ldsX + 3 * XS;
    const int nst = (nk16 + 3) >> 2;
    const bool halfLast = (nk16 & 3) != 0;
    unsigned xoff, woff;
    if (XMODE == 0) { const int r = lane >> 3, c = (lane & 7) ^ ((4 * (wave & 1) + (r >> 1)) & 7); xoff = r * ldx * 2 + c * 16; }
    else xoff = lane * 16;
    { const int r = lane >> 3, c = (lane & 7) ^ (((r >> 1) & 1) << 2); woff = r * ldw * 2 + c * 16; }
    auto issue = [&](int st, int buf) {
        const bool part = halfLast && (st == nst - 1);
        if (XMODE == 0) {
#pragma unroll
            for (int i = 0; i < XP; ++i) { const int rg = wave + 4 * i;
                glds16(X + (size_t)rg * 8 * ldx * 2 + st * 128, xoff, ldsX + buf * XS + rg * 1024); }
        } else {
            if (!(part && wave >= 2)) {
#pragma unroll
                for (int i = 0; i < XP; ++i)
                    glds16(X + ((size_t)i * ldx + st * 4 + wave) * 1024, xoff, ldsX + buf * XS + i * 4096 + wave * 1024);
            }
        }
#pragma unroll
        for (int i = 0; i < 2; ++i) {
            if (!(part && i == 1)) { const int rg = wave + 4 * i;
#pragma unroll
                for (int p = 0; p < 2; ++p)
                    glds16(W + ((size_t)st * 64 + rg * 8) * ldw * 2 + p * 128, woff, ldsW + buf * WSZ + p * 8192 + rg * 1024); }
        }
    };
    const int wt = wave & 1, wf = wave >> 1, h = lane >> 5, r32 = lane & 31;
    ldsp xb[4];
    if (XMODE == 0) {
        const int row = wt * (BT / 2) + r32; const unsigned a0 = row * 128 + ((h ^ ((row >> 1) & 7)) << 4);
#pragma unroll
        for (int kk = 0; kk < 4; ++kk) xb[kk] = lds + (a0 ^ (kk << 5));
    } else {
#pragma unroll
        for (int kk = 0; kk < 4; ++kk) xb[kk] = lds + wt * NTT * 4096 + kk * 1024 + lane * 16;
    }
    ldsp wb[2];
    { const int blk = (lane >> 4) & 1, q = (lane & 15) >> 2, pp = lane & 3;
      const int hr = (XMODE == 0) ? 8 * h : 4 * h;
#pragma unroll
      for (int fh = 0; fh < 2; ++fh)
          wb[fh] = lds + 3 * XS + wf * 8192 + (hr + q) * 128 + (((fh * 4 + blk * 2 + (pp >> 1)) ^ ((q >> 1) << 2)) << 4) + (pp & 1) * 8; }
    constexpr int TOFF = (XMODE == 0) ? 512 : 1024;
    auto compute = [&](int buf, int nkk) {
#pragma unroll
        for (int kk = 0; kk < 4; ++kk) {
            if (kk < nkk) {
                s16x8 xf[NTT], wfr[2];
#pragma unroll
                for (int tt = 0; tt < NTT; ++tt) xf[tt] = ld128(xb[kk] + buf * XS + tt * 4096);
#pragma unroll
                for (int fh = 0; fh < 2; ++fh) wfr[fh] = cat(ldtr(wb[fh] + buf * WSZ + kk * 2048), ldtr(wb[fh] + buf * WSZ + kk * 2048 + TOFF));
#pragma unroll
                for (int tt = 0; tt < NTT; ++tt)
#pragma unroll
                    for (int fh = 0; fh < 2; ++fh) acc[tt][fh] = mfma16(wfr[fh], xf[tt], acc[tt][fh]);
            }
        }
    };
    issue(0, 0); issue(1, 1);
    int st = 0;
#define GSTEP(B)                                                                             \
    {                                                                                        \
        if (st + 2 < nst || (st + 2 == nst && !halfLast)) { if (XP == 2) WAIT_BAR(6); else WAIT_BAR(8); } \
        else WAIT_BAR(0);                                                                    \
        if (st + 2 < nst) issue(st + 2, (B + 2) % 3);                                        \
        if (halfLast && st == nst - 1) compute(B, 2); else compute(B, 4);                    \
        if (++st == nst) break;                                                              \
    }
    for (;;) { GSTEP(0) GSTEP(1) GSTEP(2) }
#undef GSTEP
    asm volatile("s_waitcnt lgkmcnt(0)\n\ts_barrier" ::: "memory");
}

constexpr int K1_LDS = 3 * 16384 + 3 * 16384;
__global__ __launch_bounds__(256, 1) void k1_proj(const u16* __restrict__ x, const u16* __restrict__ wq, const u16* __restrict__ wk,
                                                  const u16* __restrict__ wv, const u16* __restrict__ w_in, const u16* __restrict__ gate_w,
                                                  char* __restrict__ ws) {
    extern __shared__ __attribute__((aligned(16))) char smem[];
    ldsp lds = (ldsp)smem;
    const int lane = threadIdx.x & 63, wave = __builtin_amdgcn_readfirstlane(threadIdx.x >> 6);
    const int xcd = blockIdx.x & 7, idx = blockIdx.x >> 3;
    const int h = lane >> 5, r32 = lane & 31;
    if (blockIdx.x == 0 && threadIdx.x < 64) ((unsigned*)(ws + WS_FLAG))[threadIdx.x * 16] = 0u;
    if (idx < 24) {
        const int panel = 2 * xcd + idx / 12, blk = idx % 12;
        const bool isq = blk < 5;
        const int col0 = isq ? blk * 128 : (FH - 32 + (blk - 5) * 128);
        f32x16 acc[2][2] = {};
        gemm_core<128, 0>(acc, (const char*)(x + (size_t)panel * 128 * D), D, (const char*)((isq ? wq : w_in) + col0), isq ? D : F2, D / 16, lds, wave, lane);
        const int wt = wave & 1, wf = wave >> 1;
#pragma unroll
        for (int tt = 0; tt < 2; ++tt) {
            const int t = panel * 128 + wt * 64 + tt * 32 + r32;
#pragma unroll
            for (int fh = 0; fh < 2; ++fh) {
                const int c = col0 + wf * 64 + fh * 32 + 4 * h;
                if (isq) {
                    const float sc = 0.22360679775f * 1.44269504089f;
                    u16* dst = (u16*)(ws + WS_Q) + (size_t)t * D + c;
#pragma unroll
                    for (int g = 0; g < 4; ++g)
                        *(u32x2*)(dst + 8 * g) = (u32x2){pk2(acc[tt][fh][4 * g] * sc, acc[tt][fh][4 * g + 1] * sc), pk2(acc[tt][fh][4 * g + 2] * sc, acc[tt][fh][4 * g + 3] * sc)};
                } else if (c >= FH) {
                    u16* dst = (u16*)(ws + WS_Z2R) + (size_t)t * ZW + (c - FH);
#pragma unroll
                    for (int g = 0; g < 4; ++g)
                        *(u32x2*)(dst + 8 * g) = (u32x2){pk2(gelu_t(acc[tt][fh][4 * g]), gelu_t(acc[tt][fh][4 * g + 1])), pk2(gelu_t(acc[tt][fh][4 * g + 2]), gelu_t(acc[tt][fh][4 * g + 3]))};
                }
            }
        }
    } else {
        const int tile = 8 * xcd + (idx - 24);
        const unsigned l0 = (unsigned)(uintptr_t)lds;
        constexpr int LWK = 40960, LWV = LWK + 25600, LRED = 0;
        { const int r = lane >> 3, c = (lane & 7) ^ ((4 * (wave & 1) + (r >> 1)) & 7);
          const unsigned xoff = r * D * 2 + c * 16;
          const char* xs = (const char*)(x + (size_t)(tile * 32 + wave * 8) * D);
#pragma unroll
          for (int st = 0; st < 10; ++st) glds16(xs + st * 128, xoff, l0 + st * 4096 + wave * 1024);
          for (int p = wave; p < 25; p += 4) { glds16((const char*)wk + p * 1024, lane * 16, l0 + LWK + p * 1024); glds16((const char*)wv + p * 1024, lane * 16, l0 + LWV + p * 1024); } }
        WAIT_BAR(0);
        f32x16 ak = {}, av = {};
        float gs = 0.f;
        const int blk = (lane >> 4) & 1, q = (lane & 15) >> 2, pp = lane & 3;
        const int quad = min(blk * 4 + pp, 4);
#pragma unroll
        for (int i = 0; i < 10; ++i) {
            const int ks = wave * 10 + i, st = ks >> 2, kk = ks & 3;
            const s16x8 xf = ld128(lds + st * 4096 + r32 * 128 + (((2 * kk + h) ^ ((r32 >> 1) & 7)) << 4));
            const int row = ks * 16 + 8 * h + q;
            const s16x8 kf = cat(ldtr(lds + LWK + row * 40 + quad * 8), ldtr(lds + LWK + (row + 4) * 40 + quad * 8));
            const s16x8 vf = cat(ldtr(lds + LWV + row * 40 + quad * 8), ldtr(lds + LWV + (row + 4) * 40 + quad * 8));
            ak = mfma16(kf, xf, ak);
            av = mfma16(xf, vf, av);
            const u32x4 gw = *(const u32x4*)(gate_w + ks * 16 + 8 * h);
            const u32x4 xu = __builtin_bit_cast(u32x4, xf);
#pragma unroll
            for (int j = 0; j < 4; ++j) gs += bflo(xu[j]) * bflo(gw[j]) + bfhi(xu[j]) * bfhi(gw[j]);
        }
        asm volatile("s_waitcnt lgkmcnt(0)\n\ts_barrier" ::: "memory");
        float* red = (float*)(smem + LRED);
#pragma unroll
        for (int r = 0; r < 16; ++r) { red[(wave * 33 + r) * 64 + lane] = ak[r]; red[(wave * 33 + 16 + r) * 64 + lane] = av[r]; }
        red[(wave * 33 + 32) * 64 + lane] = gs;
        __syncthreads();
        char* img = ws + WS_KV + (size_t)tile * KVT;
        if (wave == 0) {
            float v[12];
#pragma unroll
            for (int r = 0; r < 12; ++r) v[r] = red[r * 64 + lane] + red[(33 + r) * 64 + lane] + red[(66 + r) * 64 + lane] + red[(99 + r) * 64 + lane];
            *(u32x4*)(img + h * 512 + r32 * 16) = (u32x4){pk2(v[0], v[1]), pk2(v[2], v[3]), pk2(v[4], v[5]), pk2(v[6], v[7])};
            if (h == 0) { *(unsigned*)(img + 2304 + r32 * 4) = pk2(v[8], v[9]); *(unsigned*)(img + 2304 + 128 + r32 * 4) = pk2(v[10], v[11]); }
        } else if (wave < 3) {
            const int s = wave - 1;
            float v[8];
#pragma unroll
            for (int r = 0; r < 8; ++r) { const int rr = 16 + 8 * s + r; v[r] = red[rr * 64 + lane] + red[(33 + rr) * 64 + lane] + red[(66 + rr) * 64 + lane] + red[(99 + rr) * 64 + lane]; }
            if (r32 < 20) *(u32x4*)(img + 1024 + s * 640 + h * 320 + r32 * 16) = (u32x4){pk2(v[0], v[1]), pk2(v[2], v[3]), pk2(v[4], v[5]), pk2(v[6], v[7])};
        } else {
            float g = red[32 * 64 + lane] + red[(33 + 32) * 64 + lane] + red[(66 + 32) * 64 + lane] + red[(99 + 32) * 64 + lane];
            g += __shfl_xor(g, 32);
            if (h == 0) ((float*)(ws + WS_GATE))[tile * 32 + r32] = 1.0f / (1.0f + __expf(-g));
        }
    }
}

constexpr int CH = 8 * KVT;
DI float xhalf_max(float m) {
    auto rr = __builtin_amdgcn_permlane32_swap(__float_as_uint(m), __float_as_uint(m), false, false);
    return __builtin_fmaxf(__uint_as_float(rr[0]), __uint_as_float(rr[1]));
}
DI float max16(const f32x16& s) {
    float a = __builtin_fmaxf(s[0], s[1]);
#pragma unroll
    for (int r = 2; r < 16; ++r) a = __builtin_fmaxf(a, s[r]);
    return a;
}
DI unsigned neg_m_slots(float m, int h) {
    const unsigned hi = pk2(-m, 0.f) & 0xffffu;
    const float r = -m - bflo(hi);
    const unsigned lo = pk2(r, 0.f) & 0xffffu;
    return h == 0 ? (hi | (lo << 16)) : 0u;
}

DI void attention(const char* ws_q, const char* kv, char* ws_o, int tok0, char* smem, int wave, int lane) {
    ldsp lds = (ldsp)smem;
    const unsigned l0 = (unsigned)(uintptr_t)lds;
    const int h = lane >> 5, r32 = lane & 31;
    auto dma = [&](int c) {
#pragma unroll
        for (int i = 0; i < 5; ++i) glds16(kv + (size_t)c * CH + (wave * 5 + i) * 1024, lane * 16, l0 + (c & 1) * CH + (wave * 5 + i) * 1024);
    };
    dma(0);
    const int ta = tok0 + wave * 2;
    const char* qa = ws_q + (size_t)ta * (D * 2) + r32 * 40;
    const u32x2 a0 = *(const u32x2*)(qa + 8 * h), a1 = *(const u32x2*)(qa + 16 + 8 * h);
    const u32x2 b0 = *(const u32x2*)(qa + D * 2 + 8 * h), b1 = *(const u32x2*)(qa + D * 2 + 16 + 8 * h);
    const s16x8 qa1 = __builtin_bit_cast(s16x8, (u32x4){a0[0], a0[1], a1[0], a1[1]});
    const s16x8 qb1 = __builtin_bit_cast(s16x8, (u32x4){b0[0], b0[1], b1[0], b1[1]});
    u32x2 qa2 = {*(const unsigned*)(qa + 32 + 4 * h), 0u}, qb2 = {*(const unsigned*)(qa + D * 2 + 32 + 4 * h), 0u};
#pragma unroll
    for (int i = 0; i < 5; ++i) *(u32x4*)(smem + 2 * CH + (i * 256 + wave * 64 + lane) * 16) = (u32x4){0x3f803f80u, 0x3f803f80u, 0x3f803f80u, 0x3f803f80u};
    const unsigned kone = h == 0 ? 0x3f803f80u : 0u;
    const f32x16 zero = {};
    f32x16 oa, ob;
    for (int attempt = 0;; ++attempt) {
        dma(1);
        WAIT_BAR(5);
        ldsp kp = lds + h * 512 + r32 * 16, k2p = lds + 2304 + h * 128 + r32 * 4;
        ldsp vp = lds + 1024 + h * 320 + (r32 < 20 ? r32 * 16 : 2 * CH + (r32 < 28 ? 128 : 192));
        const int vstep = r32 < 20 ? CH : 0;
        s16x8 k1 = ld128(kp);
        u32x2 k2 = {*(const __attribute__((address_space(3))) unsigned*)k2p, kone};
        s16x8 v0 = ld128(vp), v1a = ld128(vp + 640), v1b;
        if (attempt == 0) {
            f32x16 s = mfma8(k2, qa2, mfma16(k1, qa1, zero));
            f32x16 t = mfma8(k2, qb2, mfma16(k1, qb1, zero));
            qa2[1] = neg_m_slots(xhalf_max(max16(s)), h);
            qb2[1] = neg_m_slots(xhalf_max(max16(t)), h);
        }
        oa = zero; ob = zero;
        f32x16 sa = mfma8(k2, qa2, mfma16(k1, qa1, zero)), sb;
        u32x4 p0, p1 = {0u, 0u, 0u, 0u};
        v1b = v1a;
#define EX(X, i) X[i] = __builtin_amdgcn_exp2f(X[i])
#define EX4(X, i) do { EX(X, i); EX(X, i + 1); EX(X, i + 2); EX(X, i + 3); } while (0)
#define UNIT(CUR, NXT, QN1, QN2, O, PVO, VPREV, AFTER_M1, AFTER_M3)                                                        \
        NXT = mfma16(k1, QN1, zero); EX4(CUR, 0); PIN(CUR); SBAR();                                                         \
        NXT = mfma8(k2, QN2, NXT); SBAR(); AFTER_M1; EX4(CUR, 4); p0[0] = pk2(CUR[0], CUR[1]); p0[1] = pk2(CUR[2], CUR[3]); PIN(CUR); PIN(p0); SBAR(); \
        PVO = mfma16(VPREV, __builtin_bit_cast(s16x8, p1), PVO); p0[2] = pk2(CUR[4], CUR[5]); p0[3] = pk2(CUR[6], CUR[7]); PIN(p0); SBAR(); EX4(CUR, 8); PIN(CUR); SBAR(); \
        O = mfma16(v0, __builtin_bit_cast(s16x8, p0), O); SBAR(); AFTER_M3; EX4(CUR, 12); PIN(CUR); SBAR();                  \
        p1[0] = pk2(CUR[8], CUR[9]); p1[1] = pk2(CUR[10], CUR[11]); p1[2] = pk2(CUR[12], CUR[13]); p1[3] = pk2(CUR[14], CUR[15]); PIN(p1); SBAR();
#define LDK(off) do { k1 = ld128(kp + (off)); k2[0] = *(const __attribute__((address_space(3))) unsigned*)(k2p + (off)); SBAR(); } while (0)
#define LDV(V1, off) do { v0 = ld128(vp + (off)); V1 = ld128(vp + (off) + 640); SBAR(); } while (0)
#define TILE(J, V1C, V1P)                                                                                                   \
        UNIT(sa, sb, qb1, qb2, oa, ob, V1P, LDK(((J) + 1) * KVT), (void)0)                                                  \
        UNIT(sb, sa, qa1, qa2, ob, oa, V1C, (void)0, LDV(V1P, ((J) + 1) * KVT))
        for (int c = 0; c < 8; ++c) {
            TILE(0, v1a, v1b) TILE(1, v1b, v1a) TILE(2, v1a, v1b) TILE(3, v1b, v1a) TILE(4, v1a, v1b) TILE(5, v1b, v1a) TILE(6, v1a, v1b)
            const int dk = (c & 1) ? -CH : CH, dv = (c & 1) ? -vstep : vstep;
            UNIT(sa, sb, qb1, qb2, oa, ob, v1a, { WAIT_BAR(0); if (c + 2 < 8) dma(c + 2); kp += dk; k2p += dk; vp += dv; LDK(0); }, (void)0)
            UNIT(sb, sa, qa1, qa2, ob, oa, v1b, (void)0, LDV(v1a, 0))
        }
        ob = mfma16(v1b, __builtin_bit_cast(s16x8, p1), ob);
#undef TILE
#undef UNIT
#undef LDK
#undef LDV
        if (attempt == 1) break;
        const float la = h ? oa[8] : oa[12], lb = h ? ob[8] : ob[12];
        float mx = __builtin_fmaxf(la, lb) * 0x1p28f;
#pragma unroll
        for (int r = 0; r < 12; ++r) mx = __builtin_fmaxf(mx, __builtin_fmaxf(__builtin_fabsf(oa[r]), __builtin_fabsf(ob[r])));
        const bool bad = !(mx < 0x1p127f) || !(la > 0.f) || !(lb > 0.f);
        asm volatile("s_waitcnt lgkmcnt(0)\n\ts_barrier" ::: "memory");
        if (!__syncthreads_or(bad)) break;
        qa2[1] = 0u; qb2[1] = 0u;
        float ma = -__builtin_inff(), mb = -__builtin_inff();
        for (int j = 0; j < 64; ++j) {
            const s16x8 g1 = *(const s16x8*)(kv + (size_t)j * KVT + h * 512 + r32 * 16);
            const u32x2 g2 = {*(const unsigned*)(kv + (size_t)j * KVT + 2304 + h * 128 + r32 * 4), kone};
            ma = __builtin_fmaxf(ma, max16(mfma8(g2, qa2, mfma16(g1, qa1, zero))));
            mb = __builtin_fmaxf(mb, max16(mfma8(g2, qb2, mfma16(g1, qb1, zero))));
        }
        qa2[1] = neg_m_slots(xhalf_max(ma), h);
        qb2[1] = neg_m_slots(xhalf_max(mb), h);
        dma(0);
    }
    const float ia = 1.0f / (h ? oa[8] : oa[12]), ib = 1.0f / (h ? ob[8] : ob[12]);
    char* da = ws_o + (size_t)ta * (D * 2) + r32 * 40 + 8 * h;
#pragma unroll
    for (int g = 0; g < 3; ++g) {
        if (g < 2 || h == 0) {
            *(u32x2*)(da + 16 * g) = (u32x2){pk2(oa[4 * g] * ia, oa[4 * g + 1] * ia), pk2(oa[4 * g + 2] * ia, oa[4 * g + 3] * ia)};
            *(u32x2*)(da + D * 2 + 16 * g) = (u32x2){pk2(ob[4 * g] * ib, ob[4 * g + 1] * ib), pk2(ob[4 * g + 2] * ib, ob[4 * g + 3] * ib)};
        }
    }
}

constexpr int K2_LDS = 3 * 8192 + 3 * 16384;
DI float wave_sum(float v) {
#pragma unroll
    for (int o = 32; o >= 1; o >>= 1) v += __shfl_xor(v, o);
    return v;
}
__global__ __launch_bounds__(256, 2) void k2_mix(const u16* __restrict__ x, const u16* __restrict__ w_in, const u16* __restrict__ w_sp,
                                                 const u16* __restrict__ b_sp, char* __restrict__ ws) {
    extern __shared__ __attribute__((aligned(16))) char smem[];
    ldsp lds = (ldsp)smem;
    const int lane = threadIdx.x & 63, wave = __builtin_amdgcn_readfirstlane(threadIdx.x >> 6);
    if (blockIdx.x >= 256) {
        attention(ws + WS_Q, ws + WS_KV, ws + WS_O, (blockIdx.x - 256) * 8, smem, wave, lane);
        return;
    }
    __builtin_amdgcn_s_setprio(2);
    gu32* flag = (gu32*)(ws + WS_FLAG);
    {
        const auto rsrc = __builtin_amdgcn_make_buffer_rsrc(ws + WS_Z2N, 0, S * ZW * 2, 0x00020000);
        u32x4 v[2][2];
        const int row0 = blockIdx.x * 8 + wave * 2;
#pragma unroll
        for (int i = 0; i < 2; ++i) {
            const u32x4* src = (const u32x4*)(ws + WS_Z2R + (size_t)(row0 + i) * (ZW * 2));
            v[i][0] = src[lane];
            v[i][1] = lane < 44 ? src[64 + lane] : (u32x4){0u, 0u, 0u, 0u};
        }
#pragma unroll
        for (int i = 0; i < 2; ++i) {
            float f[16];
#pragma unroll
            for (int j = 0; j < 8; ++j) { f[2 * j] = bflo(v[i][j >> 2][j & 3]); f[2 * j + 1] = bfhi(v[i][j >> 2][j & 3]); }
            float s = 0.f;
#pragma unroll
            for (int j = 0; j < 16; ++j) s += f[j];
            const float mean = wave_sum(s) * (1.0f / FH);
            float q = 0.f;
#pragma unroll
            for (int j = 0; j < 16; ++j) { f[j] -= mean; if (j < 8 || lane < 44) q += f[j] * f[j]; }
            const float rs = __builtin_amdgcn_rsqf(wave_sum(q) * (1.0f / FH) + 1e-5f);
            u32x4 o0, o1;
#pragma unroll
            for (int j = 0; j < 4; ++j) { o0[j] = pk2(f[2 * j] * rs, f[2 * j + 1] * rs); o1[j] = lane < 44 ? pk2(f[8 + 2 * j] * rs, f[9 + 2 * j] * rs) : 0u; }
            __builtin_amdgcn_raw_buffer_store_b128(o0, rsrc, (row0 + i) * (ZW * 2) + lane * 16, 0, 16);
            if (lane < 48) __builtin_amdgcn_raw_buffer_store_b128(o1, rsrc, (row0 + i) * (ZW * 2) + 1024 + lane * 16, 0, 16);
        }
        asm volatile("s_waitcnt vmcnt(0)" ::: "memory");
        __syncthreads();
        if (threadIdx.x == 0) __hip_atomic_fetch_add(flag + (blockIdx.x & 7) * 64, 1u, __ATOMIC_RELAXED, __HIP_MEMORY_SCOPE_AGENT);
    }
    const int xcd = blockIdx.x & 7, idx = blockIdx.x >> 3;
    if (idx >= 28) return;
    const int panel = 4 * xcd + idx / 7, fb = idx % 7;
    const int wt = wave & 1, wf = wave >> 1, h = lane >> 5, r32 = lane & 31;
    u32x4 z1[2][2];
    {
        f32x16 acc[1][2] = {};
        gemm_core<64, 0>(acc, (const char*)(x + (size_t)panel * 64 * D), D, (const char*)(w_in + fb * 128), F2, D / 16, lds, wave, lane);
#pragma unroll
        for (int fh = 0; fh < 2; ++fh)
#pragma unroll
            for (int j = 0; j < 8; ++j) z1[fh][j >> 2][j & 3] = pk2(gelu_t(acc[0][fh][2 * j]), gelu_t(acc[0][fh][2 * j + 1]));
    }
    if (wave == 0) {
        if (lane < 8) { while (__hip_atomic_load(flag + lane * 64, __ATOMIC_RELAXED, __HIP_MEMORY_SCOPE_AGENT) < 32u) __builtin_amdgcn_s_sleep(4); }
        __builtin_amdgcn_fence(__ATOMIC_ACQUIRE, "agent");
        asm volatile("s_waitcnt vmcnt(0)" ::: "memory");
    }
    __syncthreads();
    f32x16 acc[1][2] = {};
    gemm_core<64, 0>(acc, (const char*)(w_sp + (size_t)panel * 64 * S), S, ws + WS_Z2N + fb * 256, ZW, S / 16, lds, wave, lane);
    const int t = panel * 64 + wt * 32 + r32;
    const float bias = bflo(b_sp[t]);
#pragma unroll
    for (int fh = 0; fh < 2; ++fh) {
        const int ft = fb * 4 + wf * 2 + fh;
        if (ft < 27) {
#pragma unroll
            for (int s = 0; s < 2; ++s) {
                u32x4 o;
#pragma unroll
                for (int j = 0; j < 4; ++j)
                    o[j] = pk2(bflo(z1[fh][s][j]) * (acc[0][fh][8 * s + 2 * j] + bias), bfhi(z1[fh][s][j]) * (acc[0][fh][8 * s + 2 * j + 1] + bias));
                *(u32x4*)(ws + WS_U + ((size_t)(panel * 2 + wt) * 54 + ft * 2 + s) * 1024 + lane * 16) = o;
            }
        }
    }
}

__global__ __launch_bounds__(256, 2) void k3_out(const u16* __restrict__ wo, const u16* __restrict__ w_out, char* __restrict__ ws, u16* __restrict__ out) {
    extern __shared__ __attribute__((aligned(16))) char smem[];
    ldsp lds = (ldsp)smem;
    const int lane = threadIdx.x & 63, wave = __builtin_amdgcn_readfirstlane(threadIdx.x >> 6);
    const int xcd = blockIdx.x & 7, idx = blockIdx.x >> 3;
    const int panel = 4 * xcd + idx / 5, nb = idx % 5;
    const int wt = wave & 1, wf = wave >> 1, h = lane >> 5, r32 = lane & 31;
    f32x16 a[1][2] = {}, b[1][2] = {};
    gemm_core<64, 0>(a, ws + WS_O + (size_t)panel * 64 * D * 2, D, (const char*)(wo + nb * 128), D, D / 16, lds, wave, lane);
    gemm_core<64, 1>(b, ws + WS_U + (size_t)panel * 2 * 54 * 1024, 54, (const char*)(w_out + nb * 128), D, FH / 16, lds, wave, lane);
    const int t = panel * 64 + wt * 32 + r32;
    const float g = ((const float*)(ws + WS_GATE))[t], g1 = 1.0f - g;
#pragma unroll
    for (int fh = 0; fh < 2; ++fh) {
        u16* dst = out + (size_t)t * D + nb * 128 + wf * 64 + fh * 32 + 4 * h;
#pragma unroll
        for (int q = 0; q < 4; ++q)
            *(u32x2*)(dst + 8 * q) = (u32x2){pk2(g * a[0][fh][4 * q] + g1 * b[0][fh][4 * q], g * a[0][fh][4 * q + 1] + g1 * b[0][fh][4 * q + 1]),
                                             pk2(g * a[0][fh][4 * q + 2] + g1 * b[0][fh][4 * q + 2], g * a[0][fh][4 * q + 3] + g1 * b[0][fh][4 * q + 3])};
    }
}

extern "C" void kernel_launch(void* const* d_in, const int* in_sizes, int n_in, void* d_out, int out_size, void* d_ws, size_t ws_size,
                              hipStream_t stream) {
    const u16* x = (const u16*)d_in[0];
    const u16* wq = (const u16*)d_in[1];
    const u16* wk = (const u16*)d_in[2];
    const u16* wv = (const u16*)d_in[3];
    const u16* wo = (const u16*)d_in[4];
    const u16* w_in = (const u16*)d_in[6];
    const u16* w_sp = (const u16*)d_in[7];
    const u16* b_sp = (const u16*)d_in[8];
    const u16* w_out = (const u16*)d_in[9];
    const u16* gate_w = (const u16*)d_in[10];
    char* ws = (char*)d_ws;
    (void)hipFuncSetAttribute((const void*)k1_proj, hipFuncAttributeMaxDynamicSharedMemorySize, K1_LDS);
    (void)hipFuncSetAttribute((const void*)k2_mix, hipFuncAttributeMaxDynamicSharedMemorySize, K2_LDS);
    (void)hipFuncSetAttribute((const void*)k3_out, hipFuncAttributeMaxDynamicSharedMemorySize, K2_LDS);
    k1_proj<<<256, 256, K1_LDS, stream>>>(x, wq, wk, wv, w_in, gate_w, ws);
    k2_mix<<<512, 256, K2_LDS, stream>>>(x, w_in, w_sp, b_sp, ws);
    k3_out<<<160, 256, K2_LDS, stream>>>(wo, w_out, ws, (u16*)d_out);
}
```

```cpp
#include <hip/hip_runtime.h>
#include <hip/hip_bf16.h>
#include <stdint.h>

typedef short  s16x8  __attribute__((ext_vector_type(8)));
typedef short  s16x4  __attribute__((ext_vector_type(4)));
typedef __bf16 bh8    __attribute__((ext_vector_type(8)));
typedef __bf16 bh2    __attribute__((ext_vector_type(2)));
typedef float  f32x16 __attribute__((ext_vector_type(16)));
typedef float  f32x2  __attribute__((ext_vector_type(2)));
typedef unsigned u32x4 __attribute__((ext_vector_type(4)));
typedef unsigned u32x2 __attribute__((ext_vector_type(2)));
typedef unsigned short u16;
typedef unsigned long long u64;
typedef __attribute__((address_space(3))) char* ldsp;
typedef __attribute__((address_space(1))) unsigned gu32;
typedef __attribute__((address_space(1))) u64 gu64;

#define DI __device__ __forceinline__
#define SBAR() __builtin_amdgcn_sched_barrier(0)
#define PIN(x) asm volatile("" : "+v"(x))
#define USE(x) asm volatile("" :: "v"(x))
#define BAR() asm volatile("s_barrier" ::: "memory")
#define NOP (void)0

#define STAMP(i) do {} while (0)

#define ROT1 1
#define PF 48
constexpr int S = 2048, D = 640, FH = 864, F2 = 1728, ZW = 896;
constexpr int KVT = 2688;
constexpr int KVG = 3 * KVT;
constexpr int NKS = 96;
constexpr int NAG = 2;
constexpr unsigned WS_EPOCH = 0, WS_F1K = 512, WS_F1P = 1024, WS_CNT = 4096;
constexpr unsigned WS_GATE = 16384;
constexpr unsigned WS_KV   = WS_GATE + 8192;
constexpr unsigned WS_Q    = WS_KV + 68 * KVT;
constexpr unsigned WS_Z2R  = WS_Q + S * D * 2;
constexpr unsigned WS_Z2N  = WS_Z2R + S * ZW * 2;
constexpr unsigned WS_A    = WS_Z2N + S * ZW * 2;
constexpr unsigned WS_END  = WS_A + 64 * NKS * 1024;
constexpr int L_X = 0, L_W = 32768, L_KV = 98304, L_LNR = 131072, L_LNT = L_LNR + 14336, L_MISC = L_LNT + 14336, LDS_BYTES = L_MISC + 64 + 256;

DI f32x16 mfma16(s16x8 a, s16x8 b, f32x16 c) {
    return __builtin_amdgcn_mfma_f32_32x32x16_bf16(__builtin_bit_cast(bh8, a), __builtin_bit_cast(bh8, b), c, 0, 0, 0);
}
DI f32x16 mfma8(u32x2 a, u32x2 b, f32x16 c) {
    return __builtin_amdgcn_mfma_f32_32x32x8bf16_1k(__builtin_bit_cast(s16x4, a), __builtin_bit_cast(s16x4, b), c, 0, 0, 0);
}
DI unsigned pk2(float lo, float hi) { f32x2 p = {lo, hi}; return __builtin_bit_cast(unsigned, __builtin_convertvector(p, bh2)); }
DI float bflo(unsigned u) { return __uint_as_float(u << 16); }
DI float bfhi(unsigned u) { return __uint_as_float(u & 0xffff0000u); }
DI float gelu_t(float x) {
    const float k0 = -2.302208198f;
    const float k1 = k0 * 0.044715f;
    float u = x * __builtin_fmaf(x * x, k1, k0);
    return x * __builtin_amdgcn_rcpf(1.0f + __builtin_amdgcn_exp2f(u));
}
DI s16x8 ld128(ldsp p) { return *(const __attribute__((address_space(3))) s16x8*)p; }
DI unsigned ld32(ldsp p) { return *(const __attribute__((address_space(3))) unsigned*)p; }
DI s16x4 ldtr(ldsp p) { return __builtin_amdgcn_ds_read_tr16_b64_v4i16((__attribute__((address_space(3))) s16x4*)p); }
DI s16x8 cat(s16x4 a, s16x4 b) { return __builtin_shufflevector(a, b, 0, 1, 2, 3, 4, 5, 6, 7); }
template <int V> struct IC { static constexpr int value = V; };
template <int N> DI void wait_v_bar() { asm volatile("s_waitcnt vmcnt(%0)\n\ts_barrier" :: "i"(N) : "memory"); }
DI void wait_l1_bar() { asm volatile("s_waitcnt lgkmcnt(1)\n\ts_barrier" ::: "memory"); }
DI void wait_l_bar() { asm volatile("s_waitcnt lgkmcnt(0)\n\ts_barrier" ::: "memory"); }
DI void wait_all_bar() { asm volatile("s_waitcnt vmcnt(0) lgkmcnt(0)\n\ts_barrier" ::: "memory"); }
DI void drain() { asm volatile("s_waitcnt vmcnt(0)" ::: "memory"); }
template <int LOFF, int GOFF, bool COH>
DI void dma(const void* sbase, unsigned voff, unsigned lds) {
    if (COH) asm volatile("s_add_u32 m0, %2, %3\n\ts_nop 0\n\tglobal_load_lds_dwordx4 %0, %1 offset:%4 sc1" :: "v"(voff), "s"(sbase), "s"(lds), "i"(LOFF - GOFF), "i"(GOFF) : "memory", "scc");
    else asm volatile("s_add_u32 m0, %2, %3\n\ts_nop 0\n\tglobal_load_lds_dwordx4 %0, %1 offset:%4" :: "v"(voff), "s"(sbase), "s"(lds), "i"(LOFF - GOFF), "i"(GOFF) : "memory", "scc");
}
DI void touch(const void* sbase, unsigned voff, unsigned lds) {
    asm volatile("s_mov_b32 m0, %2\n\ts_nop 0\n\tglobal_load_lds_dword %0, %1" :: "v"(voff), "s"(sbase), "s"(lds) : "memory");
}
DI float wave_sum(float v) {
    v += __builtin_bit_cast(float, __builtin_amdgcn_update_dpp(0, __builtin_bit_cast(int, v), 0x128, 0xf, 0xf, false));
    v += __builtin_bit_cast(float, __builtin_amdgcn_update_dpp(0, __builtin_bit_cast(int, v), 0x124, 0xf, 0xf, false));
    v += __builtin_bit_cast(float, __builtin_amdgcn_update_dpp(0, __builtin_bit_cast(int, v), 0x122, 0xf, 0xf, false));
    v += __builtin_bit_cast(float, __builtin_amdgcn_update_dpp(0, __builtin_bit_cast(int, v), 0x121, 0xf, 0xf, false));
    auto a = __builtin_amdgcn_permlane16_swap(__float_as_uint(v), __float_as_uint(v), false, false);
    v = __uint_as_float(a[0]) + __uint_as_float(a[1]);
    auto b = __builtin_amdgcn_permlane32_swap(__float_as_uint(v), __float_as_uint(v), false, false);
    return __uint_as_float(b[0]) + __uint_as_float(b[1]);
}
DI float xhalf_max(float m) {
    auto rr = __builtin_amdgcn_permlane32_swap(__float_as_uint(m), __float_as_uint(m), false, false);
    return __builtin_fmaxf(__uint_as_float(rr[0]), __uint_as_float(rr[1]));
}
DI float max16(const f32x16& s) {
    float a = __builtin_fmaxf(s[0], s[1]);
#pragma unroll
    for (int r = 2; r < 16; ++r) a = __builtin_fmaxf(a, s[r]);
    return a;
}
DI unsigned neg_m_slots(float m, int h) {
    const unsigned hi = pk2(-m, 0.f) & 0xffffu;
    const float r = -m - bflo(hi);
    const unsigned lo = pk2(r, 0.f) & 0xffffu;
    return h == 0 ? (hi | (lo << 16)) : 0u;
}
DI bool spin_over(u64 t0) { __builtin_amdgcn_s_sleep(6); return __builtin_amdgcn_s_memrealtime() - t0 > 20000000ull; }
DI void poll_count(const char* p, unsigned want) {
    const u64 t0 = __builtin_amdgcn_s_memrealtime();
    while (__any(__hip_atomic_load((gu32*)p, __ATOMIC_RELAXED, __HIP_MEMORY_SCOPE_AGENT) < want)) if (spin_over(t0)) break;
    asm volatile("" ::: "memory");
}
DI void poll_tags(const char* p, const char* q, u64 tag) {
    const u64 t0 = __builtin_amdgcn_s_memrealtime();
    for (;;) {
        const u64 a = __hip_atomic_load((gu64*)p, __ATOMIC_RELAXED, __HIP_MEMORY_SCOPE_AGENT), b = __hip_atomic_load((gu64*)q, __ATOMIC_RELAXED, __HIP_MEMORY_SCOPE_AGENT);
        if (__all(a == tag && b == tag) || spin_over(t0)) break;
    }
    asm volatile("" ::: "memory");
}

template <int NTT> struct Slots;
template <> struct Slots<1> { static constexpr int X(int s) { return L_X + s * 8192; } static constexpr int W(int s) { return L_W + s * 16384; } };
template <> struct Slots<2> {
    static constexpr int X(int s) { return 16384 * (s == 0 ? 1 : s == 1 ? 6 : s == 2 ? 0 : 3); }
    static constexpr int W(int s) { return 16384 * (s == 0 ? 4 : s == 1 ? 7 : s == 2 ? 2 : 5); } };
template <int NTT, int XMODE, int NSTEP, class HOOK>
DI void gemm_compute(f32x16 (&acc)[NTT][2], ldsp lds, int wave, int lane, HOOK hook) {
    const int wt = wave & 1, wf = wave >> 1, h = lane >> 5, r32 = lane & 31;
    ldsp xb[4], wb[2];
    if (XMODE == 0) {
        const int row = wt * (32 * NTT) + r32; const unsigned a0 = row * 128 + ((h ^ ((row >> 1) & 7)) << 4);
#pragma unroll
        for (int kk = 0; kk < 4; ++kk) xb[kk] = lds + (a0 ^ (kk << 5));
    } else {
#pragma unroll
        for (int kk = 0; kk < 4; ++kk) xb[kk] = lds + (wt * NTT * 4096 + kk * 1024 + lane * 16);
    }
    { const int blk = (lane >> 4) & 1, q = (lane & 15) >> 2, pp = lane & 3;
      const int hr = (XMODE == 0) ? 8 * h : 4 * h;
#pragma unroll
      for (int fh = 0; fh < 2; ++fh)
          wb[fh] = lds + (wf * 8192 + (hr + q) * 128 + (((fh * 4 + blk * 2 + (pp >> 1)) ^ ((q >> 1) << 2)) << 4) + (pp & 1) * 8); }
    constexpr int TOFF = (XMODE == 0) ? 512 : 1024;
    s16x8 xf[4][NTT] = {}, wfr[4][2] = {};
    auto m1 = [&](int kk, int i) { acc[i >> 1][i & 1] = mfma16(wfr[kk][i & 1], xf[kk][i >> 1], acc[i >> 1][i & 1]); SBAR(); };
    auto mm = [&](int kk) {
#pragma unroll
        for (int i = 0; i < 2 * NTT; ++i) m1(kk, i);
    };
    auto step = [&](auto SL) {
        constexpr int XO = Slots<NTT>::X(SL.value), WO = Slots<NTT>::W(SL.value);
        auto rw = [&](int kk, int fh) { wfr[kk][fh] = cat(ldtr(wb[fh] + WO + kk * 2048), ldtr(wb[fh] + WO + kk * 2048 + TOFF)); SBAR(); };
        auto rx = [&](int kk, int tt) { xf[kk][tt] = ld128(xb[kk] + XO + tt * 4096); SBAR(); };
        USE(xf[3][NTT - 1]); BAR(); SBAR();
        if (NTT == 1) {
            m1(2, 0); rw(0, 0); rx(0, 0); m1(2, 1); rw(0, 1); m1(3, 0); rw(1, 0); rx(1, 0); m1(3, 1); rw(1, 1);
            m1(0, 0); rw(2, 0); rx(2, 0); m1(0, 1); rw(2, 1); rw(3, 0); m1(1, 0); rw(3, 1); rx(3, 0); m1(1, 1);
        } else {
            m1(3, 0); rw(0, 0); rx(0, 0); m1(3, 1); rw(0, 1); rx(0, 1); m1(3, 2); rw(1, 0); m1(3, 3); rx(1, 0);
            m1(0, 0); rw(1, 1); m1(0, 1); rx(1, 1); m1(0, 2); rw(2, 0); m1(0, 3); rx(2, 0);
            m1(1, 0); rw(2, 1); m1(1, 1); rx(2, 1); m1(1, 2); rw(3, 0); m1(1, 3); rx(3, 0);
            m1(2, 0); rw(3, 1); m1(2, 1); rx(3, 1); m1(2, 2); m1(2, 3);
        }
    };
#pragma unroll 1
    for (int n = 0; n < NSTEP / 4; ++n) { step(IC<0>{}); step(IC<1>{}); hook(); step(IC<2>{}); step(IC<3>{}); }
    if (NSTEP % 4 > 0) step(IC<0>{});
    if (NSTEP % 4 > 1) step(IC<1>{});
    if (NTT == 1) mm(2);
    mm(3);
}

__global__ __launch_bounds__(512, 2) void fused(const u16* __restrict__ x, const u16* __restrict__ wq, const u16* __restrict__ wk,
                                                const u16* __restrict__ wv, const u16* __restrict__ wo, const u16* __restrict__ w_in,
                                                const u16* __restrict__ w_sp, const u16* __restrict__ b_sp, const u16* __restrict__ w_out,
                                                const u16* __restrict__ gate_w, char* ws, u16* __restrict__ out) {
    extern __shared__ __attribute__((aligned(16))) char smem[];
    u64 pc0; asm volatile("s_getpc_b64 %0" : "=s"(pc0));
    ldsp lds = (ldsp)smem;
    const unsigned l0 = (unsigned)(uintptr_t)lds;
    const int lane = threadIdx.x & 63, wave8 = __builtin_amdgcn_readfirstlane(threadIdx.x >> 6), wave = wave8 & 3;
    const int xcd = blockIdx.x & 7, idx = blockIdx.x >> 3;
    const int h = lane >> 5, r32 = lane & 31, wt = wave & 1, wf = wave >> 1;
    const bool big = idx < 24, hasg = idx < 28, haso = idx < 20;
    const int p1 = 4 * (xcd >> 1) + idx / 6, blk1 = 2 * (idx % 6) + (xcd & 1);
    const bool isq = blk1 < 5;
    const int col1 = isq ? blk1 * 128 : (FH - 32 + (blk1 - 5) * 128);
    const int panel = 4 * xcd + idx / 7, fb = idx % 7;
    const int po = 4 * xcd + idx / 5, nb = idx % 5;
    const int tok0 = xcd * 256 + idx * 8;
    const char* kv = ws + WS_KV;
    volatile int* badl = (volatile int*)(smem + L_MISC);
    const auto wsr = __builtin_amdgcn_make_buffer_rsrc(ws, 0, WS_END, 0x00020000);
#define STAUX 16
#define ST16(v, off) __builtin_amdgcn_raw_buffer_store_b128(v, wsr, off, 0, STAUX)
#define ST4(v, off) __builtin_amdgcn_raw_buffer_store_b32(v, wsr, off, 0, 16)
    STAMP(0);

    auto layernorm = [&]() {
        u32x4 v[2];
        v[0] = *(const u32x4*)(smem + L_LNR + lane * 128 + wave8 * 16);
        v[1] = lane < 44 ? *(const u32x4*)(smem + L_LNR + (64 + lane) * 128 + wave8 * 16) : (u32x4){0u, 0u, 0u, 0u};
        float f[16];
#pragma unroll
        for (int j = 0; j < 8; ++j) { f[2 * j] = gelu_t(bflo(v[j >> 2][j & 3])); f[2 * j + 1] = gelu_t(bfhi(v[j >> 2][j & 3])); }
        float s = 0.f;
#pragma unroll
        for (int j = 0; j < 16; ++j) s += f[j];
        const float mean = wave_sum(s) * (1.0f / FH);
        float q = 0.f;
#pragma unroll
        for (int j = 0; j < 16; ++j) { f[j] -= mean; if (j < 8 || lane < 44) q += f[j] * f[j]; }
        const float rs = __builtin_amdgcn_rsqf(wave_sum(q) * (1.0f / FH) + 1e-5f);
        const float r1 = lane < 44 ? rs : 0.f;
        char* dst = smem + L_LNT + wave8 * (ZW * 2) + (lane >> 1) * 32 + (lane & 1) * 8;
        *(u32x2*)dst = (u32x2){pk2(f[0] * rs, f[1] * rs), pk2(f[2] * rs, f[3] * rs)};
        *(u32x2*)(dst + 16) = (u32x2){pk2(f[4] * rs, f[5] * rs), pk2(f[6] * rs, f[7] * rs)};
        if (lane < 48) {
            *(u32x2*)(dst + 1024) = (u32x2){pk2(f[8] * r1, f[9] * r1), pk2(f[10] * r1, f[11] * r1)};
            *(u32x2*)(dst + 1040) = (u32x2){pk2(f[12] * r1, f[13] * r1), pk2(f[14] * r1, f[15] * r1)};
        }
    };

    if (wave8 >= 4) {
        const unsigned lq = l0 + wave * 1024;
        const int r = lane >> 3;
        if (wave == 0 && lane == 0) *(unsigned*)(smem + L_MISC + 16) = 0u;
        const unsigned csw = ((lane & 7) ^ ((4 * (wave & 1) + (r >> 1)) & 7)) << 4, wsw = ((lane & 7) ^ (((r >> 1) & 1) << 2)) << 4;
        auto wpieces = [&](const char* wp, unsigned o0, unsigned o1, unsigned lw) {
            dma<0, 0, false>(wp, o0, lw); dma<8192, 128, false>(wp, o0, lw); dma<4096, 0, false>(wp, o1, lw); dma<12288, 128, false>(wp, o1, lw); };
        const unsigned zx = (wave * 8 + r) * (D * 2) + csw, zw = (wave * 8 + r) * (F2 * 2) + wsw;
        auto zstage = [&](int n) {
            const char* xp = (const char*)(x + (size_t)panel * 64 * D) + n * 128;
            const unsigned lx = lq + L_X + (n & 3) * 8192;
            dma<0, 0, false>(xp, zx, lx); dma<4096, 0, false>(xp, zx + 32 * D * 2, lx);
            wpieces((const char*)(w_in + fb * 128) + (size_t)n * (64 * F2 * 2), zw, zw + 32 * F2 * 2, lq + L_W + (n & 3) * 16384);
        };
        u64 tag;
        if (big) {
            const int ldw = isq ? D : F2;
            const unsigned pw = (wave * 8 + r) * ldw * 2 + wsw;
            const char* xp0 = (const char*)(x + (size_t)p1 * 128 * D); const char* wp0 = (const char*)((isq ? wq : w_in) + col1);
            int st = ROT1 * (((idx / 6) * 5 / 2 + (idx % 6) * 5 / 3) % 10);
            auto pstage = [&](auto SL) {
                const char* xp = xp0 + st * 128;
                const unsigned lx = lq + Slots<2>::X(SL.value);
                dma<0, 0, false>(xp, zx, lx); dma<4096, 0, false>(xp, zx + 32 * D * 2, lx); dma<8192, 0, false>(xp, zx + 64 * D * 2, lx); dma<12288, 0, false>(xp, zx + 96 * D * 2, lx);
                wpieces(wp0 + (size_t)st * 64 * ldw * 2, pw, pw + 32 * ldw * 2, lq + Slots<2>::W(SL.value));
                st = st == 9 ? 0 : st + 1;
            };
            pstage(IC<0>{}); pstage(IC<1>{}); pstage(IC<2>{});
            asm volatile("s_load_dwordx2 %0, %1, 0x0\n\ts_waitcnt lgkmcnt(0)" : "=s"(tag) : "s"(ws) : "memory");
            STAMP(8);
            wait_v_bar<16>(); STAMP(9); pstage(IC<3>{}); wait_v_bar<16>(); pstage(IC<0>{}); wait_v_bar<16>(); pstage(IC<1>{}); wait_v_bar<16>(); pstage(IC<2>{});
            wait_v_bar<16>(); STAMP(10); pstage(IC<3>{}); wait_v_bar<16>(); pstage(IC<0>{}); wait_v_bar<16>(); pstage(IC<1>{});
            wait_v_bar<16>(); zstage(0); wait_v_bar<14>(); zstage(1); wait_v_bar<12>(); zstage(2);
        } else {
            const int tile = 16 * (xcd >> 1) + 8 * (xcd & 1) + (idx - 24);
            const char* xs = (const char*)(x + (size_t)(tile * 32) * D);
#pragma unroll
            for (int st = 0; st < 10; ++st) dma<0, 0, false>(xs + st * 128, zx, lq + st * 4096);
            for (int p = wave; p < 25; p += 4) { dma<40960, 0, false>((const char*)wk + p * 1024, lane * 16, l0 + p * 1024); dma<66560, 0, false>((const char*)wv + p * 1024, lane * 16, l0 + p * 1024); }
            if (blockIdx.x == 255 && wave == 0) { ST4(0u, WS_CNT + lane * 128); if (lane < 8) ST4(0u, WS_CNT + (64 + lane) * 128); }
            asm volatile("s_load_dwordx2 %0, %1, 0x0\n\ts_waitcnt lgkmcnt(0)" : "=s"(tag) : "s"(ws) : "memory");
            wait_v_bar<0>(); BAR(); BAR();
            if (hasg) { zstage(0); zstage(1); zstage(2); }
        }
        tag += 1;
        STAMP(1);
        auto publish1 = [&]() { if (wave == 0 && lane == 0) __hip_atomic_store((gu64*)(ws + (big ? WS_F1P + (p1 * 16 + blk1) * 8 : WS_F1K + (16 * (xcd >> 1) + 8 * (xcd & 1) + idx - 24) * 8)), tag, __ATOMIC_RELAXED, __HIP_MEMORY_SCOPE_AGENT); };
        auto kvpiece = [&](int g, int i) { dma<L_KV, 0, true>(kv + (size_t)g * KVG + i * 4096, wave * 1024 + lane * 16, lq + (g & 3) * 8192 + i * 4096); };
        auto wait_f1 = [&]() { if (wave == 0) poll_tags(ws + WS_F1K + lane * 8, ws + WS_F1P + ((tok0 >> 7) * 16 + min(lane, 11)) * 8, tag); };
        if (!big) publish1();
        if (hasg) {
#pragma unroll 1
            for (int n = 0; n < 7; ++n) { wait_v_bar<12>(); if (n == 2 && big) publish1(); zstage(n + 3); }
            STAMP(11);
            wait_v_bar<12>(); drain(); wait_f1(); BAR();
        } else { wait_f1(); BAR(); }
        STAMP(12);
        {
            const char* zr = ws + WS_Z2R + (size_t)(tok0 >> 5) * (27 * 2048) + (tok0 & 31) * 16;
#pragma unroll
            for (int i = 0; i < 4; ++i) if (i < 3 || wave < 2) dma<L_LNR, 0, true>(zr + (wave + 4 * i) * 4096, r * 512 + (lane & 7) * 16, lq + i * 4096);
        }
        if (hasg) BAR();
#pragma unroll
        for (int g = 0; g < 4; ++g) { kvpiece(g, 0); kvpiece(g, 1); }
        if (PF & 8) touch((const char*)w_sp + (size_t)xcd * (256 * S * 2) + (idx * 4 + wave) * 8192, lane * 128, l0 + L_MISC + 64);
        STAMP(2);
        wait_v_bar<8 + ((PF & 8) ? 1 : 0)>();
        layernorm();
        asm volatile("s_waitcnt vmcnt(%0) lgkmcnt(0)\n\ts_barrier" :: "i"(4 + ((PF & 8) ? 1 : 0)) : "memory");
#pragma unroll
        for (int it = 0; it < 4; ++it) {
            const int cb = wave + 4 * it;
            if (cb < 14) {
                ldsp p = lds + L_LNT + ((lane & 15) >> 2) * (ZW * 2) + (cb * 64 + (lane >> 4) * 16 + (lane & 3) * 4) * 2;
                const s16x8 c = cat(ldtr(p), ldtr(p + 4 * (ZW * 2)));
                ST16(__builtin_bit_cast(u32x4, c), WS_Z2N + ((cb * 2 + h) * 128 + (tok0 >> 4)) * 1024 + ((tok0 >> 3) & 1) * 512 + r32 * 16);
            }
        }
        drain();
        if (lane == 0 && __hip_atomic_fetch_add((__attribute__((address_space(3))) unsigned*)(lds + L_MISC + 16), 1u, __ATOMIC_RELAXED, __HIP_MEMORY_SCOPE_WORKGROUP) == 3u)
            __hip_atomic_fetch_add((gu32*)(ws + WS_CNT + xcd * 128), 1u, __ATOMIC_RELAXED, __HIP_MEMORY_SCOPE_AGENT);
        STAMP(6);
        const unsigned gx = (wave * 8 + r) * (S * 2) + csw;
        auto gstage = [&](int n) {
            const unsigned lx = lq + L_X + (n & 3) * 8192, lw = lq + L_W + (n & 3) * 16384;
            const char* xp = (const char*)(w_sp + (size_t)panel * 64 * S) + n * 128; const char* wp = ws + WS_Z2N + (size_t)fb * 4 * 131072 + n * 4096;
            const unsigned wo_ = wave * 1024 + lane * 16;
            dma<0, 0, false>(xp, gx, lx); dma<4096, 0, false>(xp, gx + 32 * S * 2, lx);
            dma<0, 0, true>(wp, wo_, lw); dma<4096, 0, true>(wp + 131072, wo_, lw); dma<8192, 0, true>(wp + 2 * 131072, wo_, lw); dma<12288, 0, true>(wp + 3 * 131072, wo_, lw);
        };
        const unsigned ow = (wave * 8 + r) * (D * 2) + wsw;
        auto ostage_w = [&](int n) {
            const char* wp = n < 14 ? (const char*)(w_out + nb * 128) + (size_t)n * (64 * D * 2) : (const char*)(wo + nb * 128) + (size_t)(n - 14) * (64 * D * 2);
            wpieces(wp, ow, n == 13 ? ow : ow + 32 * D * 2, lq + L_W + (n & 3) * 16384);
        };
        auto ostage_x = [&](int n) {
            const char* xp = ws + WS_A + (size_t)po * 2 * NKS * 1024 + n * 4096;
            const unsigned lx = lq + L_X + (n & 3) * 8192, xo_ = wave * 1024 + lane * 16;
            dma<0, 0, true>(xp, xo_, lx); dma<4096, 0, true>(xp + NKS * 1024, xo_, lx);
        };
        auto add1 = [&](int c) { if (wave == 0 && lane == 0) __hip_atomic_fetch_add((gu32*)(ws + WS_CNT + c * 128), 1u, __ATOMIC_RELAXED, __HIP_MEMORY_SCOPE_AGENT); };
        for (int attempt = 0;; ++attempt) {
            const bool weave = attempt == 0 && hasg;
            const int gend = weave ? NAG : 21;
#pragma unroll 1
            for (int g = 1; g < gend; ++g) {
                if (g + 2 > 21) wait_v_bar<0>(); else wait_v_bar<2>();
                if (g + 3 <= 21) { kvpiece(g + 3, 0); kvpiece(g + 3, 1); }
            }
            if (weave) {
                poll_count(ws + WS_CNT + (lane & 7) * 128, 32u);
                if (blockIdx.x == 0 && wave == 0 && lane == 0)
                    __hip_atomic_store((gu64*)(ws + WS_EPOCH), tag, __ATOMIC_RELAXED, __HIP_MEMORY_SCOPE_AGENT);
                STAMP(3);
                gstage(0); gstage(1); gstage(2);
#pragma unroll 1
                for (int n = 0; n < 32; ++n) {
                    if (n < 30) wait_v_bar<12>(); else if (n == 30) wait_v_bar<8>(); else wait_v_bar<2>();
                    if (n + 3 < 32) gstage(n + 3);
                    kvpiece(NAG + 3 + (n >> 1), n & 1);
                    if ((PF & 16) && n == 22) { const int w_ = idx * 4 + wave;
                        touch((const char*)w_out + w_ * 8192, lane * 128, l0 + L_LNT + wave * 256);
                        if (w_ < 23) touch((const char*)w_out + (112 + w_) * 8192, lane * 128, l0 + L_LNT + wave * 256);
                        if (w_ >= 12) touch((const char*)wo + (w_ - 12) * 8192, lane * 128, l0 + L_LNT + wave * 256); }
                }
                static_assert(NAG == 2, "the tail below is written for groups 18, 19, 20");
                wait_v_bar<2>();
                kvpiece(21, 0); kvpiece(21, 1);
                if (haso) { ostage_w(0); ostage_w(1); ostage_w(2); wait_v_bar<14>(); } else wait_v_bar<2>();
                add1(8 + panel);
                if (haso) wait_v_bar<12>(); else wait_v_bar<0>();
                if (haso) { poll_count(ws + WS_CNT + (8 + po) * 128, 7u);
                    if (PF & 32) { const int w_ = nb * 4 + wave; if (w_ < 14) touch(ws + WS_A + (size_t)(po * 2 + (w_ & 1)) * NKS * 1024 + (w_ >> 1) * 8192, lane * 128, l0 + L_LNT + wave * 256); }
                    ostage_x(0); ostage_x(1); ostage_x(2); }
                STAMP(4);
            }
            wait_l_bar();
            if (attempt == 1 || !__builtin_amdgcn_readfirstlane(badl[0] | badl[1] | badl[2] | badl[3])) break;
            BAR();
#pragma unroll
            for (int gg = 0; gg < 4; ++gg) { kvpiece(gg, 0); kvpiece(gg, 1); }
            wait_v_bar<0>();
        }
#pragma unroll
        for (int it = 0; it < 3; ++it) {
            const int id = it * 256 + wave * 64 + lane;
            if (it < 2 || wave < 2) ST16(*(const u32x4*)(smem + L_LNR + id * 16), WS_A + ((tok0 >> 5) * NKS + 56) * 1024 + (id >> 3) * 512 + ((tok0 & 31) + (id & 7)) * 16);
        }
        if (!haso) { wait_v_bar<0>(); add1(40 + (tok0 >> 6)); return; }
        wait_v_bar<4>(); ostage_x(3); ostage_w(3);
        wait_v_bar<6>(); add1(40 + (tok0 >> 6)); ostage_x(4); ostage_w(4);
#pragma unroll 1
        for (int n = 2; n < 21; ++n) {
            wait_v_bar<12>();
            if (n + 3 == 14) { poll_count(ws + WS_CNT + (40 + po) * 128, 8u); STAMP(5);
                if (PF & 32) { const int w_ = nb * 4 + wave; if (w_ < 10) touch(ws + WS_A + (size_t)((po * 2 + (w_ & 1)) * NKS + 56) * 1024 + (w_ >> 1) * 8192, lane * 128, l0 + L_LNT + wave * 256); } }
            ostage_x(n + 3); ostage_w(n + 3);
        }
        wait_v_bar<12>(); wait_v_bar<6>(); wait_v_bar<0>();
        return;
    }

    const unsigned lscr = l0 + L_LNT + wave * 256;
    const int wid = idx * 4 + wave;
    if (PF & 1) {
        if (wid < 5) touch((const char*)(uintptr_t)(pc0 & ~127ull) + wid * 8192, lane * 128, lscr);
    }
    if (PF & 2) {
        if (wid < 80) touch(x + (size_t)(xcd >> 1) * 512 * D, (wid * 64 + lane) * 128, lscr);
        if (wid < 120) { const int b6 = wid / 20, ln = (wid % 20) * 64 + lane, rw_ = ln >> 1, bb = 2 * b6 + (xcd & 1);
            const u16* base = bb < 5 ? wq : w_in;
            const unsigned off = bb < 5 ? rw_ * (D * 2) + bb * 256 + (ln & 1) * 128 : rw_ * (F2 * 2) + (FH - 32 + (bb - 5) * 128) * 2 + (ln & 1) * 128;
            touch(base, off, lscr); }
    }
    if (PF & 4) {
        { const int ln = wid * 64 + lane; touch(w_in, (ln / 14) * (F2 * 2) + (ln % 14) * 128, lscr); }
        if (wid < 12) { const int ln = (128 + wid) * 64 + lane; touch(w_in, (ln / 14) * (F2 * 2) + (ln % 14) * 128, lscr); }
    }
    if (big) {
        f32x16 acc[2][2] = {};
        gemm_compute<2, 0, 10>(acc, lds, wave, lane, [] {});
        STAMP(1);
        const float sc = isq ? 0.22360679775f * 1.44269504089f : 1.0f;
#pragma unroll
        for (int tt = 0; tt < 2; ++tt) {
            const int tb = p1 * 4 + wt * 2 + tt;
#pragma unroll
            for (int fh = 0; fh < 2; ++fh) {
                const int c = col1 + wf * 64 + fh * 32;
                if (isq || c >= FH) {
                    const unsigned o = (isq ? WS_Q + (tb * 20 + (c >> 5)) * 2048 : WS_Z2R + (tb * 27 + ((c - FH) >> 5)) * 2048) + lane * 16;
#pragma unroll
                    for (int s = 0; s < 2; ++s) {
                        const f32x16& a = acc[tt][fh];
                        ST16(((u32x4){pk2(a[8 * s] * sc, a[8 * s + 1] * sc), pk2(a[8 * s + 2] * sc, a[8 * s + 3] * sc),
                                      pk2(a[8 * s + 4] * sc, a[8 * s + 5] * sc), pk2(a[8 * s + 6] * sc, a[8 * s + 7] * sc)}), o + s * 1024);
                    }
                }
            }
        }
    } else {
        const int tile = 16 * (xcd >> 1) + 8 * (xcd & 1) + (idx - 24);
        constexpr int LWK = 40960, LWV = LWK + 25600;
        u32x4 gw[10];
#pragma unroll
        for (int i = 0; i < 10; ++i) gw[i] = *(const u32x4*)(gate_w + (wave * 10 + i) * 16 + 8 * h);
        BAR();
        f32x16 ak = {}, av = {};
        float gs = 0.f;
        const int blk = (lane >> 4) & 1, q = (lane & 15) >> 2, pp = lane & 3;
        const int quad = min(blk * 4 + pp, 4);
#pragma unroll
        for (int i = 0; i < 10; ++i) {
            const int ks = wave * 10 + i, st = ks >> 2, kk = ks & 3;
            const s16x8 xf = ld128(lds + st * 4096 + r32 * 128 + (((2 * kk + h) ^ ((r32 >> 1) & 7)) << 4));
            const int row = ks * 16 + 8 * h + q;
            const s16x8 kf = cat(ldtr(lds + LWK + row * 40 + quad * 8), ldtr(lds + LWK + (row + 4) * 40 + quad * 8));
            const s16x8 vf = cat(ldtr(lds + LWV + row * 40 + quad * 8), ldtr(lds + LWV + (row + 4) * 40 + quad * 8));
            ak = mfma16(kf, xf, ak);
            av = mfma16(xf, vf, av);
            const u32x4 xu = __builtin_bit_cast(u32x4, xf);
#pragma unroll
            for (int j = 0; j < 4; ++j) gs += bflo(xu[j]) * bflo(gw[i][j]) + bfhi(xu[j]) * bfhi(gw[i][j]);
        }
        float* red = (float*)(smem + L_KV);
#pragma unroll
        for (int r = 0; r < 16; ++r) { red[(wave * 33 + r) * 64 + lane] = ak[r]; red[(wave * 33 + 16 + r) * 64 + lane] = av[r]; }
        red[(wave * 33 + 32) * 64 + lane] = gs;
        wait_l_bar();
        const unsigned img = WS_KV + tile * KVT;
        auto rsum = [&](int r) { return red[r * 64 + lane] + red[(33 + r) * 64 + lane] + red[(66 + r) * 64 + lane] + red[(99 + r) * 64 + lane]; };
        if (wave == 0) {
            float v[12];
#pragma unroll
            for (int r = 0; r < 12; ++r) v[r] = rsum(r);
            ST16(((u32x4){pk2(v[0], v[1]), pk2(v[2], v[3]), pk2(v[4], v[5]), pk2(v[6], v[7])}), img + h * 512 + r32 * 16);
            if (h == 0) { ST4(pk2(v[8], v[9]), img + 2304 + r32 * 4); ST4(pk2(v[10], v[11]), img + 2304 + 128 + r32 * 4); }
        } else if (wave < 3) {
            const int s = wave - 1;
            float v[8];
#pragma unroll
            for (int r = 0; r < 8; ++r) v[r] = rsum(16 + 8 * s + r);
            if (r32 < 20) ST16(((u32x4){pk2(v[0], v[1]), pk2(v[2], v[3]), pk2(v[4], v[5]), pk2(v[6], v[7])}), img + 1024 + s * 640 + h * 320 + r32 * 16);
        } else {
            float g = rsum(32);
            auto rr = __builtin_amdgcn_permlane32_swap(__float_as_uint(g), __float_as_uint(g), false, false);
            g = __uint_as_float(rr[0]) + __uint_as_float(rr[1]);
            if (h == 0) ST4(__float_as_uint(1.0f / (1.0f + __expf(-g))), WS_GATE + (tile * 32 + r32) * 4);
            if (lane < 2) { const unsigned c = lane ? 0u : 0x3f803f80u; ST16(((u32x4){c, c, c, c}), img + 2560 + lane * 16); }
        }
        wait_all_bar();
    }
    STAMP(2);
    f32x16 acc[1][2] = {};
    u32x4 z1[2][2];
    if (hasg) gemm_compute<1, 0, 10>(acc, lds, wave, lane, [] { drain(); });
    else BAR();
    STAMP(3);
    s16x8 qa1, qb1; u32x2 qa2, qb2; float gate_a, gate_b, g1; unsigned braw;
    {
        const int ta = tok0 + wave * 2;
        auto qoff = [&](int qd) { return WS_Q + ((ta >> 5) * 20 + (qd >> 3)) * 2048 + ((qd >> 2) & 1) * 1024 + (qd & 1) * 512 + (ta & 31) * 16 + ((qd >> 1) & 1) * 8; };
        const unsigned o0 = qoff(5 * r32 + h), o1 = qoff(5 * r32 + 2 + h), o2 = qoff(5 * r32 + 4) + 4 * h;
        const u32x2 a0 = __builtin_amdgcn_raw_buffer_load_b64(wsr, o0, 0, 16), a1 = __builtin_amdgcn_raw_buffer_load_b64(wsr, o1, 0, 16);
        const u32x2 b0 = __builtin_amdgcn_raw_buffer_load_b64(wsr, o0 + 16, 0, 16), b1 = __builtin_amdgcn_raw_buffer_load_b64(wsr, o1 + 16, 0, 16);
        qa1 = __builtin_bit_cast(s16x8, (u32x4){a0[0], a0[1], a1[0], a1[1]});
        qb1 = __builtin_bit_cast(s16x8, (u32x4){b0[0], b0[1], b1[0], b1[1]});
        qa2 = (u32x2){__builtin_amdgcn_raw_buffer_load_b32(wsr, o2, 0, 16), 0u};
        qb2 = (u32x2){__builtin_amdgcn_raw_buffer_load_b32(wsr, o2 + 16, 0, 16), 0u};
        gate_a = __uint_as_float(__builtin_amdgcn_raw_buffer_load_b32(wsr, WS_GATE + ta * 4, 0, 16));
        gate_b = __uint_as_float(__builtin_amdgcn_raw_buffer_load_b32(wsr, WS_GATE + ta * 4 + 4, 0, 16));
        const int t = (hasg ? panel : 0) * 64 + wt * 32 + r32;
        braw = b_sp[t]; g1 = __uint_as_float(__builtin_amdgcn_raw_buffer_load_b32(wsr, WS_GATE + t * 4, 0, 16));
        SBAR();
    }
    if (hasg) {
#pragma unroll
        for (int fh = 0; fh < 2; ++fh) {
#pragma unroll
            for (int j = 0; j < 8; ++j) z1[fh][j >> 2][j & 3] = pk2(gelu_t(acc[0][fh][2 * j]), gelu_t(acc[0][fh][2 * j + 1]));
            acc[0][fh] = (f32x16){};
        }
    }
    STAMP(4);
    BAR();
    STAMP(13);
    layernorm();
    wait_l_bar();
    STAMP(5);
    PIN(qa1); PIN(qb1); PIN(qa2); PIN(qb2); PIN(gate_a); PIN(gate_b); PIN(braw); PIN(g1);
    const float bias = bflo(braw); g1 = 1.0f - g1;
    const unsigned kone = h == 0 ? 0x3f803f80u : 0u;
    const f32x16 zero = {};
    ldsp xb[4], wb;
    { const int row = wt * 32 + r32; const unsigned a0 = row * 128 + ((h ^ ((row >> 1) & 7)) << 4);
#pragma unroll
      for (int kk = 0; kk < 4; ++kk) xb[kk] = lds + L_X + (a0 ^ (kk << 5));
      wb = lds + L_W + wf * 8192 + lane * 16; }
    for (int attempt = 0;; ++attempt) {
        const int oc = (r32 == 20 || r32 == 24) ? 2560 : 2576;
        ldsp kp = lds + L_KV + h * 512 + r32 * 16, k2p = lds + L_KV + 2304 + h * 128 + r32 * 4;
        ldsp vp0 = lds + L_KV + (r32 < 20 ? 1024 + h * 320 + r32 * 16 : oc), vp1 = vp0 + (r32 < 20 ? 640 : 0);
        s16x8 k1 = ld128(kp); u32x2 k2 = {ld32(k2p), kone};
        s16x8 v0 = ld128(vp0), v1 = v0, v0b;
        if (attempt == 0) {
            qa2[1] = neg_m_slots(xhalf_max(max16(mfma8(k2, qa2, mfma16(k1, qa1, zero)))), h);
            qb2[1] = neg_m_slots(xhalf_max(max16(mfma8(k2, qb2, mfma16(k1, qb1, zero)))), h);
        }
        f32x16 oa = zero, ob = zero;
        f32x16 sa = mfma8(k2, qa2, mfma16(k1, qa1, zero)), sb;
        u32x4 p0, p1 = {0u, 0u, 0u, 0u};
        s16x8 xf[4], wf0[4], wf1[4];
        xf[2] = (s16x8){}; xf[3] = (s16x8){}; wf1[2] = (s16x8){}; wf0[3] = (s16x8){}; wf1[3] = (s16x8){};
        STAMP(6);
#define B8(v) __builtin_bit_cast(s16x8, v)
#define EX(X, i) X[i] = __builtin_amdgcn_exp2f(X[i])
#define LDK(off) do { k1 = ld128(kp + (off)); k2[0] = ld32(k2p + (off)); } while (0)
#define LDV0(off) v0 = ld128(vp0 + (off))
#define LDV1(off) v1 = ld128(vp1 + (off))
#define C0(i, CUR) p0[i] = pk2(CUR[2 * (i)], CUR[2 * (i) + 1])
#define C1(i, CUR) p1[i] = pk2(CUR[8 + 2 * (i)], CUR[9 + 2 * (i)])
#define GAP(MF, RD, CUR, BODY) MF; SBAR(); RD; SBAR(); BODY; PIN(CUR); SBAR();
#define U4(CUR, NXT, QN1, QN2, O, PVO, R1, R2, R3)                                                                              \
        GAP(NXT = mfma16(k1, QN1, zero), NOP, CUR, { EX(CUR, 0); EX(CUR, 1); EX(CUR, 2); EX(CUR, 3); })                         \
        GAP(NXT = mfma8(k2, QN2, NXT), R1, CUR, { EX(CUR, 4); EX(CUR, 5); EX(CUR, 6); EX(CUR, 7); C0(0, CUR); C0(1, CUR); PIN(p0); }) \
        GAP(PVO = mfma16(v1, B8(p1), PVO), R2, CUR, { C0(2, CUR); C0(3, CUR); PIN(p0); SBAR(); EX(CUR, 8); EX(CUR, 9); EX(CUR, 10); EX(CUR, 11); }) \
        GAP(O = mfma16(v0, B8(p0), O), R3, CUR, { EX(CUR, 12); EX(CUR, 13); EX(CUR, 14); EX(CUR, 15); C1(0, CUR); C1(1, CUR); C1(2, CUR); C1(3, CUR); PIN(p1); })
#define U7(CUR, NXT, QN1, QN2, O, PVO, V0, G0, G1, G2, R0, R1, R2, R3, R4)                                                      \
        GAP(NXT = mfma16(k1, QN1, zero), R0, CUR, { EX(CUR, 0); EX(CUR, 1); })                                                  \
        GAP(G0, R1, CUR, { EX(CUR, 2); EX(CUR, 3); })                                                                           \
        GAP(NXT = mfma8(k2, QN2, NXT), R2, CUR, { EX(CUR, 4); EX(CUR, 5); C0(0, CUR); PIN(p0); })                               \
        GAP(G1, R3, CUR, { EX(CUR, 6); EX(CUR, 7); C0(1, CUR); C0(2, CUR); PIN(p0); })                                          \
        GAP(PVO = mfma16(v1, B8(p1), PVO), R4, CUR, { EX(CUR, 8); EX(CUR, 9); C0(3, CUR); PIN(p0); })                           \
        GAP(G2, NOP, CUR, { EX(CUR, 10); EX(CUR, 11); EX(CUR, 12); C1(0, CUR); PIN(p1); })                                      \
        GAP(O = mfma16(V0, B8(p0), O), NOP, CUR, { EX(CUR, 13); EX(CUR, 14); EX(CUR, 15); C1(1, CUR); C1(2, CUR); C1(3, CUR); PIN(p1); })
#define U6(CUR, NXT, QN1, QN2, O, PVO, V0, G0, G1, R0, R1, R2, R3)                                                              \
        GAP(NXT = mfma16(k1, QN1, zero), R0, CUR, { EX(CUR, 0); EX(CUR, 1); EX(CUR, 2); })                                      \
        GAP(G0, R1, CUR, { EX(CUR, 3); EX(CUR, 4); C0(0, CUR); PIN(p0); })                                                      \
        GAP(NXT = mfma8(k2, QN2, NXT), R2, CUR, { EX(CUR, 5); EX(CUR, 6); EX(CUR, 7); C0(1, CUR); PIN(p0); })                   \
        GAP(PVO = mfma16(v1, B8(p1), PVO), R3, CUR, { EX(CUR, 8); EX(CUR, 9); C0(2, CUR); C0(3, CUR); PIN(p0); })               \
        GAP(G1, NOP, CUR, { EX(CUR, 10); EX(CUR, 11); EX(CUR, 12); C1(0, CUR); PIN(p1); })                                      \
        GAP(O = mfma16(V0, B8(p0), O), NOP, CUR, { EX(CUR, 13); EX(CUR, 14); EX(CUR, 15); C1(1, CUR); C1(2, CUR); C1(3, CUR); PIN(p1); })
#define GM(kk, f) acc[0][f] = mfma16(wf##f[kk], xf[kk], acc[0][f])
#define RX(kk, S_) xf[kk] = ld128(xb[kk] + (S_) * 8192)
#define RW(kk, f, S_) wf##f[kk] = ld128(wb + (S_) * 16384 + (f) * 4096 + (kk) * 1024)
#define A_ sa, sb, qb1, qb2, oa, ob
#define B_ sb, sa, qa1, qa2, ob, oa
#define XP(m, ...) m(__VA_ARGS__)
#define TILE(T, KN, VN) XP(U4, A_, KN, LDV1(T), NOP) XP(U4, B_, NOP, NOP, VN)
#define BODY(T, S0, S1, VA, VB, KN, VN)                                                                                          \
        USE(VA); BAR(); SBAR();                                                                                                  \
        XP(U7, A_, VA, GM(2, 1), GM(3, 0), GM(3, 1), { RX(0, S0); RW(0, 0, S0); }, { RW(0, 1, S0); RX(1, S0); }, LDK((T) + KVT), NOP, LDV1(T)) \
        USE(v1); SBAR();                                                                                                         \
        XP(U6, B_, VA, GM(0, 0), GM(0, 1), { RW(1, 0, S0); RW(1, 1, S0); }, { RX(2, S0); RW(2, 0, S0); }, VB = ld128(vp0 + (T) + KVT), NOP) \
        USE(VB); SBAR();                                                                                                         \
        XP(U7, A_, VB, GM(1, 0), GM(1, 1), GM(2, 0), { RW(2, 1, S0); RX(3, S0); }, { RW(3, 0, S0); RW(3, 1, S0); }, LDK((T) + 2 * KVT), NOP, LDV1((T) + KVT)) \
        USE(v1); BAR(); SBAR();                                                                                                  \
        XP(U7, B_, VB, GM(2, 1), GM(3, 0), GM(3, 1), { RX(0, S1); RW(0, 0, S1); }, { RW(0, 1, S1); RX(1, S1); }, VA = ld128(vp0 + (T) + 2 * KVT), NOP, NOP) \
        USE(VA); SBAR();                                                                                                         \
        XP(U6, A_, VA, GM(0, 0), GM(0, 1), { RW(1, 0, S1); RW(1, 1, S1); }, { RX(2, S1); RW(2, 0, S1); }, KN, LDV1((T) + 2 * KVT)) \
        USE(v1); SBAR();                                                                                                         \
        XP(U7, B_, VA, GM(1, 0), GM(1, 1), GM(2, 0), { RW(2, 1, S1); RX(3, S1); }, { RW(3, 0, S1); RW(3, 1, S1); }, VN, NOP, NOP)
        int g = 0;
        for (int part = 0; part < 2; ++part) {
            const bool weave = attempt == 0 && part == 0 && hasg;
            const int gend = weave ? NAG : 21;
#pragma unroll 1
            for (; g < gend; ++g) {
                const int dn = (g & 3) == 3 ? -3 * 8192 : 8192;
                if (g) wait_l1_bar();
                TILE(0, LDK(KVT), LDV0(KVT)) TILE(KVT, LDK(2 * KVT), LDV0(2 * KVT))
                drain(); SBAR();
                TILE(2 * KVT, { kp += dn; k2p += dn; LDK(0); }, { vp0 += dn; vp1 += dn; LDV0(0); })
            }
            if (!weave) break;
            STAMP(7);
#pragma unroll 1
            for (int i = 0; i < 8; ++i) {
                const int dn = ((g >> 1) & 1) ? -16384 : 16384;
                BODY(0, 0, 1, v0, v0b, LDK(8192), v0b = ld128(vp0 + 8192))
                BODY(8192, 2, 3, v0b, v0, { kp += dn; k2p += dn; LDK(0); }, { vp0 += dn; vp1 += dn; LDV0(0); })
                g += 2;
            }
            GM(2, 1); GM(3, 0); GM(3, 1);
            STAMP(8);
#pragma unroll
            for (int fh = 0; fh < 2; ++fh) {
                const int ft = fb * 4 + wf * 2 + fh;
#pragma unroll
                for (int s = 0; s < 2; ++s) {
                    u32x4 o;
#pragma unroll
                    for (int j = 0; j < 4; ++j)
                        o[j] = pk2(g1 * bflo(z1[fh][s][j]) * (acc[0][fh][8 * s + 2 * j] + bias), g1 * bfhi(z1[fh][s][j]) * (acc[0][fh][8 * s + 2 * j + 1] + bias));
                    if (ft == 27) o = (u32x4){0u, 0u, 0u, 0u};
                    ST16(o, WS_A + ((panel * 2 + wt) * NKS + ft * 2 + s) * 1024 + lane * 16);
                }
            }
        }
        TILE(0, NOP, NOP)
        ob = mfma16(v1, B8(p1), ob);
        STAMP(9);
        const float la = h ? oa[8] : oa[12], lb = h ? ob[8] : ob[12];
        float mx = __builtin_fmaxf(la, lb) * 0x1p26f;
#pragma unroll
        for (int r = 0; r < 12; ++r) mx = __builtin_fmaxf(mx, __builtin_fmaxf(__builtin_fabsf(oa[r]), __builtin_fabsf(ob[r])));
        const bool bad = __any(!(mx < 0x1p126f));
        if (lane == 0) badl[wave] = bad;
        const float ia = gate_a * __builtin_amdgcn_rcpf(la), ib = gate_b * __builtin_amdgcn_rcpf(lb);
#pragma unroll
        for (int gq = 0; gq < 3; ++gq) {
            if (gq < 2 || h == 0) {
                const int qd = 5 * r32 + 2 * gq + h;
                char* dst = smem + L_LNR + (2 * (qd >> 2) + (qd & 1)) * 128 + wave * 32 + ((qd >> 1) & 1) * 8;
                *(u32x2*)dst = (u32x2){pk2(oa[4 * gq] * ia, oa[4 * gq + 1] * ia), pk2(oa[4 * gq + 2] * ia, oa[4 * gq + 3] * ia)};
                *(u32x2*)(dst + 16) = (u32x2){pk2(ob[4 * gq] * ib, ob[4 * gq + 1] * ib), pk2(ob[4 * gq + 2] * ib, ob[4 * gq + 3] * ib)};
            }
        }
        wait_l_bar();
        if (attempt == 1 || !__builtin_amdgcn_readfirstlane(badl[0] | badl[1] | badl[2] | badl[3])) break;
        BAR();
        qa2[1] = 0u; qb2[1] = 0u;
        float ma = -__builtin_inff(), mb = -__builtin_inff();
#pragma unroll 1
        for (int j = 0; j < 64; ++j) {
            const s16x8 c1 = B8(__builtin_amdgcn_raw_buffer_load_b128(wsr, WS_KV + j * KVT + h * 512 + r32 * 16, 0, 16));
            const u32x2 c2 = {__builtin_amdgcn_raw_buffer_load_b32(wsr, WS_KV + j * KVT + 2304 + h * 128 + r32 * 4, 0, 16), kone};
            ma = __builtin_fmaxf(ma, max16(mfma8(c2, qa2, mfma16(c1, qa1, zero))));
            mb = __builtin_fmaxf(mb, max16(mfma8(c2, qb2, mfma16(c1, qb1, zero))));
        }
        qa2[1] = neg_m_slots(xhalf_max(ma), h);
        qb2[1] = neg_m_slots(xhalf_max(mb), h);
        BAR();
    }
    STAMP(10);
    if (!haso) { BAR(); return; }
    acc[0][0] = zero; acc[0][1] = zero;
    gemm_compute<1, 1, 24>(acc, lds, wave, lane, [] {});
    STAMP(11);
    const int t = po * 64 + wt * 32 + r32;
#pragma unroll
    for (int fh = 0; fh < 2; ++fh) {
        char* dst = (char*)(out + (size_t)t * D + nb * 128 + wf * 64 + fh * 32) + 16 * h;
#pragma unroll
        for (int q = 0; q < 2; ++q) {
            const f32x16& a = acc[0][fh];
            auto s0 = __builtin_amdgcn_permlane32_swap(pk2(a[8 * q], a[8 * q + 1]), pk2(a[8 * q + 4], a[8 * q + 5]), false, false);
            auto s1 = __builtin_amdgcn_permlane32_swap(pk2(a[8 * q + 2], a[8 * q + 3]), pk2(a[8 * q + 6], a[8 * q + 7]), false, false);
            *(u32x4*)(dst + 32 * q) = (u32x4){s0[0], s1[0], s0[1], s1[1]};
        }
    }
    STAMP(12);
}

extern "C" void kernel_launch(void* const* d_in, const int* in_sizes, int n_in, void* d_out, int out_size, void* d_ws, size_t ws_size,
                              hipStream_t stream) {
    (void)hipFuncSetAttribute((const void*)fused, hipFuncAttributeMaxDynamicSharedMemorySize, LDS_BYTES);
    fused<<<256, 512, LDS_BYTES, stream>>>((const u16*)d_in[0], (const u16*)d_in[1], (const u16*)d_in[2], (const u16*)d_in[3], (const u16*)d_in[4],
                                           (const u16*)d_in[6], (const u16*)d_in[7], (const u16*)d_in[8], (const u16*)d_in[9], (const u16*)d_in[10],
                                           (char*)d_ws, (u16*)d_out);
}
```

```cpp
#include <hip/hip_runtime.h>
#include <hip/hip_bf16.h>
#include <stdint.h>

typedef short  s16x8  __attribute__((ext_vector_type(8)));
typedef short  s16x4  __attribute__((ext_vector_type(4)));
typedef __bf16 bh8    __attribute__((ext_vector_type(8)));
typedef __bf16 bh2    __attribute__((ext_vector_type(2)));
typedef float  f32x16 __attribute__((ext_vector_type(16)));
typedef float  f32x2  __attribute__((ext_vector_type(2)));
typedef unsigned u32x4 __attribute__((ext_vector_type(4)));
typedef unsigned u32x2 __attribute__((ext_vector_type(2)));
typedef unsigned short u16;
typedef unsigned long long u64;
typedef __attribute__((address_space(3))) char* ldsp;
typedef __attribute__((address_space(1))) unsigned gu32;
typedef __attribute__((address_space(1))) u64 gu64;

#define DI __device__ __forceinline__
#define SBAR() __builtin_amdgcn_sched_barrier(0)
#define PIN(x) asm volatile("" : "+v"(x))
#define USE(x) asm volatile("" :: "v"(x))
#define BAR() asm volatile("s_barrier" ::: "memory")
#define NOP (void)0

#define STAMP(i) do {} while (0)

#define ROT1 1
#define PFG 15
#define PF 61
constexpr int S = 2048, D = 640, FH = 864, F2 = 1728, ZW = 896;
constexpr int KVT = 2688;
constexpr int KVG = 3 * KVT;
constexpr int NKS = 96;
constexpr int NAG = 2;
constexpr unsigned WS_EPOCH = 0, WS_F1K = 512, WS_F1P = 1024, WS_CNT = 4096;
constexpr unsigned WS_GATE = 16384;
constexpr unsigned WS_KV   = WS_GATE + 8192;
constexpr unsigned WS_Q    = WS_KV + 68 * KVT;
constexpr unsigned WS_Z2R  = WS_Q + S * D * 2;
constexpr unsigned WS_Z2N  = WS_Z2R + S * ZW * 2;
constexpr unsigned WS_A    = WS_Z2N + S * ZW * 2;
constexpr unsigned WS_END  = WS_A + 64 * NKS * 1024;
constexpr int L_X = 0, L_W = 32768, L_KV = 98304, L_LNR = 131072, L_LNT = L_LNR + 14336, L_MISC = L_LNT + 14336, LDS_BYTES = L_MISC + 64 + 256;

DI f32x16 mfma16(s16x8 a, s16x8 b, f32x16 c) {
    return __builtin_amdgcn_mfma_f32_32x32x16_bf16(__builtin_bit_cast(bh8, a), __builtin_bit_cast(bh8, b), c, 0, 0, 0);
}
DI f32x16 mfma8(u32x2 a, u32x2 b, f32x16 c) {
    return __builtin_amdgcn_mfma_f32_32x32x8bf16_1k(__builtin_bit_cast(s16x4, a), __builtin_bit_cast(s16x4, b), c, 0, 0, 0);
}
DI unsigned pk2(float lo, float hi) { f32x2 p = {lo, hi}; return __builtin_bit_cast(unsigned, __builtin_convertvector(p, bh2)); }
DI float bflo(unsigned u) { return __uint_as_float(u << 16); }
DI float bfhi(unsigned u) { return __uint_as_float(u & 0xffff0000u); }
DI float gelu_t(float x) {
    const float k0 = -2.302208198f;
    const float k1 = k0 * 0.044715f;
    float u = x * __builtin_fmaf(x * x, k1, k0);
    return x * __builtin_amdgcn_rcpf(1.0f + __builtin_amdgcn_exp2f(u));
}
DI s16x8 ld128(ldsp p) { return *(const __attribute__((address_space(3))) s16x8*)p; }
DI unsigned ld32(ldsp p) { return *(const __attribute__((address_space(3))) unsigned*)p; }
DI s16x4 ldtr(ldsp p) { return __builtin_amdgcn_ds_read_tr16_b64_v4i16((__attribute__((address_space(3))) s16x4*)p); }
DI s16x8 cat(s16x4 a, s16x4 b) { return __builtin_shufflevector(a, b, 0, 1, 2, 3, 4, 5, 6, 7); }
template <int V> struct IC { static constexpr int value = V; };
template <int N> DI void wait_v_bar() { asm volatile("s_waitcnt vmcnt(%0)\n\ts_barrier" :: "i"(N) : "memory"); }
DI void wait_l1_bar() { asm volatile("s_waitcnt lgkmcnt(1)\n\ts_barrier" ::: "memory"); }
DI void wait_l_bar() { asm volatile("s_waitcnt lgkmcnt(0)\n\ts_barrier" ::: "memory"); }
DI void wait_all_bar() { asm volatile("s_waitcnt vmcnt(0) lgkmcnt(0)\n\ts_barrier" ::: "memory"); }
DI void drain() { asm volatile("s_waitcnt vmcnt(0)" ::: "memory"); }
template <int LOFF, int GOFF, bool COH>
DI void dma(const void* sbase, unsigned voff, unsigned lds) {
    if (COH) asm volatile("s_add_u32 m0, %2, %3\n\ts_nop 0\n\tglobal_load_lds_dwordx4 %0, %1 offset:%4 sc1" :: "v"(voff), "s"(sbase), "s"(lds), "i"(LOFF - GOFF), "i"(GOFF) : "memory", "scc");
    else asm volatile("s_add_u32 m0, %2, %3\n\ts_nop 0\n\tglobal_load_lds_dwordx4 %0, %1 offset:%4" :: "v"(voff), "s"(sbase), "s"(lds), "i"(LOFF - GOFF), "i"(GOFF) : "memory", "scc");
}
DI void touch(const void* sbase, unsigned voff, unsigned lds) {
    asm volatile("s_mov_b32 m0, %2\n\ts_nop 0\n\tglobal_load_lds_dword %0, %1" :: "v"(voff), "s"(sbase), "s"(lds) : "memory");
}
DI float wave_sum(float v) {
    v += __builtin_bit_cast(float, __builtin_amdgcn_update_dpp(0, __builtin_bit_cast(int, v), 0x128, 0xf, 0xf, false));
    v += __builtin_bit_cast(float, __builtin_amdgcn_update_dpp(0, __builtin_bit_cast(int, v), 0x124, 0xf, 0xf, false));
    v += __builtin_bit_cast(float, __builtin_amdgcn_update_dpp(0, __builtin_bit_cast(int, v), 0x122, 0xf, 0xf, false));
    v += __builtin_bit_cast(float, __builtin_amdgcn_update_dpp(0, __builtin_bit_cast(int, v), 0x121, 0xf, 0xf, false));
    auto a = __builtin_amdgcn_permlane16_swap(__float_as_uint(v), __float_as_uint(v), false, false);
    v = __uint_as_float(a[0]) + __uint_as_float(a[1]);
    auto b = __builtin_amdgcn_permlane32_swap(__float_as_uint(v), __float_as_uint(v), false, false);
    return __uint_as_float(b[0]) + __uint_as_float(b[1]);
}
DI float xhalf_max(float m) {
    auto rr = __builtin_amdgcn_permlane32_swap(__float_as_uint(m), __float_as_uint(m), false, false);
    return __builtin_fmaxf(__uint_as_float(rr[0]), __uint_as_float(rr[1]));
}
DI float max16(const f32x16& s) {
    float a = __builtin_fmaxf(s[0], s[1]);
#pragma unroll
    for (int r = 2; r < 16; ++r) a = __builtin_fmaxf(a, s[r]);
    return a;
}
DI unsigned neg_m_slots(float m, int h) {
    const unsigned hi = pk2(-m, 0.f) & 0xffffu;
    const float r = -m - bflo(hi);
    const unsigned lo = pk2(r, 0.f) & 0xffffu;
    return h == 0 ? (hi | (lo << 16)) : 0u;
}
DI bool spin_over(u64 t0) { __builtin_amdgcn_s_sleep(6); return __builtin_amdgcn_s_memrealtime() - t0 > 20000000ull; }
DI void poll_count(const char* p, unsigned want) {
    const u64 t0 = __builtin_amdgcn_s_memrealtime();
    while (__any(__hip_atomic_load((gu32*)p, __ATOMIC_RELAXED, __HIP_MEMORY_SCOPE_AGENT) < want)) if (spin_over(t0)) break;
    asm volatile("" ::: "memory");
}
DI void poll_tags(const char* p, const char* q, u64 tag) {
    const u64 t0 = __builtin_amdgcn_s_memrealtime();
    for (;;) {
        const u64 a = __hip_atomic_load((gu64*)p, __ATOMIC_RELAXED, __HIP_MEMORY_SCOPE_AGENT), b = __hip_atomic_load((gu64*)q, __ATOMIC_RELAXED, __HIP_MEMORY_SCOPE_AGENT);
        if (__all(a == tag && b == tag) || spin_over(t0)) break;
    }
    asm volatile("" ::: "memory");
}

template <int NTT> struct Slots;
template <> struct Slots<1> { static constexpr int X(int s) { return L_X + s * 8192; } static constexpr int W(int s) { return L_W + s * 16384; } };
template <> struct Slots<2> {
    static constexpr int X(int s) { return 16384 * (s == 0 ? 1 : s == 1 ? 6 : s == 2 ? 0 : 3); }
    static constexpr int W(int s) { return 16384 * (s == 0 ? 4 : s == 1 ? 7 : s == 2 ? 2 : 5); } };
template <int NTT, int XMODE, int NSTEP, class HOOK>
DI void gemm_compute(f32x16 (&acc)[NTT][2], ldsp lds, int wave, int lane, HOOK hook) {
    const int wt = wave & 1, wf = wave >> 1, h = lane >> 5, r32 = lane & 31;
    ldsp xb[4], wb[2];
    if (XMODE == 0) {
        const int row = wt * (32 * NTT) + r32; const unsigned a0 = row * 128 + ((h ^ ((row >> 1) & 7)) << 4);
#pragma unroll
        for (int kk = 0; kk < 4; ++kk) xb[kk] = lds + (a0 ^ (kk << 5));
    } else {
#pragma unroll
        for (int kk = 0; kk < 4; ++kk) xb[kk] = lds + (wt * NTT * 4096 + kk * 1024 + lane * 16);
    }
    { const int blk = (lane >> 4) & 1, q = (lane & 15) >> 2, pp = lane & 3;
      const int hr = (XMODE == 0) ? 8 * h : 4 * h;
#pragma unroll
      for (int fh = 0; fh < 2; ++fh)
          wb[fh] = lds + (wf * 8192 + (hr + q) * 128 + (((fh * 4 + blk * 2 + (pp >> 1)) ^ ((q >> 1) << 2)) << 4) + (pp & 1) * 8); }
    constexpr int TOFF = (XMODE == 0) ? 512 : 1024;
    s16x8 xf[4][NTT] = {}, wfr[4][2] = {};
    auto m1 = [&](int kk, int i) { acc[i >> 1][i & 1] = mfma16(wfr[kk][i & 1], xf[kk][i >> 1], acc[i >> 1][i & 1]); SBAR(); };
    auto mm = [&](int kk) {
#pragma unroll
        for (int i = 0; i < 2 * NTT; ++i) m1(kk, i);
    };
    auto step = [&](auto SL) {
        constexpr int XO = Slots<NTT>::X(SL.value), WO = Slots<NTT>::W(SL.value);
        auto rw = [&](int kk, int fh) { wfr[kk][fh] = cat(ldtr(wb[fh] + WO + kk * 2048), ldtr(wb[fh] + WO + kk * 2048 + TOFF)); SBAR(); };
        auto rx = [&](int kk, int tt) { xf[kk][tt] = ld128(xb[kk] + XO + tt * 4096); SBAR(); };
        USE(xf[3][NTT - 1]); BAR(); SBAR();
        if (NTT == 1) {
            m1(2, 0); rw(0, 0); rx(0, 0); m1(2, 1); rw(0, 1); m1(3, 0); rw(1, 0); rx(1, 0); m1(3, 1); rw(1, 1);
            m1(0, 0); rw(2, 0); rx(2, 0); m1(0, 1); rw(2, 1); rw(3, 0); m1(1, 0); rw(3, 1); rx(3, 0); m1(1, 1);
        } else {
            m1(3, 0); rw(0, 0); rx(0, 0); m1(3, 1); rw(0, 1); rx(0, 1); m1(3, 2); rw(1, 0); m1(3, 3); rx(1, 0);
            m1(0, 0); rw(1, 1); m1(0, 1); rx(1, 1); m1(0, 2); rw(2, 0); m1(0, 3); rx(2, 0);
            m1(1, 0); rw(2, 1); m1(1, 1); rx(2, 1); m1(1, 2); rw(3, 0); m1(1, 3); rx(3, 0);
            m1(2, 0); rw(3, 1); m1(2, 1); rx(3, 1); m1(2, 2); m1(2, 3);
        }
    };
#pragma unroll 1
    for (int n = 0; n < NSTEP / 4; ++n) { step(IC<0>{}); step(IC<1>{}); hook(); step(IC<2>{}); step(IC<3>{}); }
    if (NSTEP % 4 > 0) step(IC<0>{});
    if (NSTEP % 4 > 1) step(IC<1>{});
    if (NTT == 1) mm(2);
    mm(3);
}

__global__ __launch_bounds__(512, 2) void fused(const u16* __restrict__ x, const u16* __restrict__ wq, const u16* __restrict__ wk,
                                                const u16* __restrict__ wv, const u16* __restrict__ wo, const u16* __restrict__ w_in,
                                                const u16* __restrict__ w_sp, const u16* __restrict__ b_sp, const u16* __restrict__ w_out,
                                                const u16* __restrict__ gate_w, char* ws, u16* __restrict__ out) {
    extern __shared__ __attribute__((aligned(16))) char smem[];
    u64 pc0; asm volatile("s_getpc_b64 %0" : "=s"(pc0));
    ldsp lds = (ldsp)smem;
    const unsigned l0 = (unsigned)(uintptr_t)lds;
    const int lane = threadIdx.x & 63, wave8 = __builtin_amdgcn_readfirstlane(threadIdx.x >> 6), wave = wave8 & 3;
    const int xcd = blockIdx.x & 7, idx = blockIdx.x >> 3;
    const int h = lane >> 5, r32 = lane & 31, wt = wave & 1, wf = wave >> 1;
    const bool big = idx < 24, hasg = idx < 28, haso = idx < 20;
    const int p1 = 4 * (xcd >> 1) + idx / 6, blk1 = 2 * (idx % 6) + (xcd & 1);
    const bool isq = blk1 < 5;
    const int col1 = isq ? blk1 * 128 : (FH - 32 + (blk1 - 5) * 128);
    const int panel = 4 * xcd + idx / 7, fb = idx % 7;
    const int po = 4 * xcd + idx / 5, nb = idx % 5;
    const int tok0 = xcd * 256 + idx * 8;
    const char* kv = ws + WS_KV;
    volatile int* badl = (volatile int*)(smem + L_MISC);
    const auto wsr = __builtin_amdgcn_make_buffer_rsrc(ws, 0, WS_END, 0x00020000);
#define STAUX 16
#define ST16(v, off) __builtin_amdgcn_raw_buffer_store_b128(v, wsr, off, 0, STAUX)
#define ST4(v, off) __builtin_amdgcn_raw_buffer_store_b32(v, wsr, off, 0, 16)
    STAMP(0);

    auto layernorm = [&]() {
        u32x4 v[2];
        v[0] = *(const u32x4*)(smem + L_LNR + lane * 128 + wave8 * 16);
        v[1] = lane < 44 ? *(const u32x4*)(smem + L_LNR + (64 + lane) * 128 + wave8 * 16) : (u32x4){0u, 0u, 0u, 0u};
        float f[16];
#pragma unroll
        for (int j = 0; j < 8; ++j) { f[2 * j] = gelu_t(bflo(v[j >> 2][j & 3])); f[2 * j + 1] = gelu_t(bfhi(v[j >> 2][j & 3])); }
        float s = 0.f;
#pragma unroll
        for (int j = 0; j < 16; ++j) s += f[j];
        const float mean = wave_sum(s) * (1.0f / FH);
        float q = 0.f;
#pragma unroll
        for (int j = 0; j < 16; ++j) { f[j] -= mean; if (j < 8 || lane < 44) q += f[j] * f[j]; }
        const float rs = __builtin_amdgcn_rsqf(wave_sum(q) * (1.0f / FH) + 1e-5f);
        const float r1 = lane < 44 ? rs : 0.f;
        char* dst = smem + L_LNT + wave8 * (ZW * 2) + (lane >> 1) * 32 + (lane & 1) * 8;
        *(u32x2*)dst = (u32x2){pk2(f[0] * rs, f[1] * rs), pk2(f[2] * rs, f[3] * rs)};
        *(u32x2*)(dst + 16) = (u32x2){pk2(f[4] * rs, f[5] * rs), pk2(f[6] * rs, f[7] * rs)};
        if (lane < 48) {
            *(u32x2*)(dst + 1024) = (u32x2){pk2(f[8] * r1, f[9] * r1), pk2(f[10] * r1, f[11] * r1)};
            *(u32x2*)(dst + 1040) = (u32x2){pk2(f[12] * r1, f[13] * r1), pk2(f[14] * r1, f[15] * r1)};
        }
    };

    if (wave8 >= 4) {
        const unsigned lq = l0 + wave * 1024;
        const int r = lane >> 3;
        if (wave == 0 && lane == 0) *(unsigned*)(smem + L_MISC + 16) = 0u;
        const unsigned csw = ((lane & 7) ^ ((4 * (wave & 1) + (r >> 1)) & 7)) << 4, wsw = ((lane & 7) ^ (((r >> 1) & 1) << 2)) << 4;
        auto wpieces = [&](const char* wp, unsigned o0, unsigned o1, unsigned lw) {
            dma<0, 0, false>(wp, o0, lw); dma<8192, 128, false>(wp, o0, lw); dma<4096, 0, false>(wp, o1, lw); dma<12288, 128, false>(wp, o1, lw); };
        const unsigned zx = (wave * 8 + r) * (D * 2) + csw, zw = (wave * 8 + r) * (F2 * 2) + wsw;
        auto zstage = [&](int n) {
            const char* xp = (const char*)(x + (size_t)panel * 64 * D) + n * 128;
            const unsigned lx = lq + L_X + (n & 3) * 8192;
            dma<0, 0, false>(xp, zx, lx); dma<4096, 0, false>(xp, zx + 32 * D * 2, lx);
            wpieces((const char*)(w_in + fb * 128) + (size_t)n * (64 * F2 * 2), zw, zw + 32 * F2 * 2, lq + L_W + (n & 3) * 16384);
        };
        u64 tag;
        if (big) {
            const int ldw = isq ? D : F2;
            const unsigned pw = (wave * 8 + r) * ldw * 2 + wsw;
            const char* xp0 = (const char*)(x + (size_t)p1 * 128 * D); const char* wp0 = (const char*)((isq ? wq : w_in) + col1);
            int st = ROT1 * (((idx / 6) * 5 / 2 + (idx % 6) * 5 / 3) % 10);
            auto pstage = [&](auto SL) {
                const char* xp = xp0 + st * 128;
                const unsigned lx = lq + Slots<2>::X(SL.value);
                dma<0, 0, false>(xp, zx, lx); dma<4096, 0, false>(xp, zx + 32 * D * 2, lx); dma<8192, 0, false>(xp, zx + 64 * D * 2, lx); dma<12288, 0, false>(xp, zx + 96 * D * 2, lx);
                wpieces(wp0 + (size_t)st * 64 * ldw * 2, pw, pw + 32 * ldw * 2, lq + Slots<2>::W(SL.value));
                st = st == 9 ? 0 : st + 1;
            };
            pstage(IC<0>{}); pstage(IC<1>{}); pstage(IC<2>{});
            asm volatile("s_load_dwordx2 %0, %1, 0x0\n\ts_waitcnt lgkmcnt(0)" : "=s"(tag) : "s"(ws) : "memory");
            STAMP(8);
            wait_v_bar<16>(); STAMP(9); pstage(IC<3>{}); wait_v_bar<16>(); pstage(IC<0>{}); wait_v_bar<16>(); pstage(IC<1>{}); wait_v_bar<16>(); pstage(IC<2>{});
            wait_v_bar<16>(); STAMP(10); pstage(IC<3>{}); wait_v_bar<16>(); pstage(IC<0>{}); wait_v_bar<16>(); pstage(IC<1>{});
            wait_v_bar<16>(); zstage(0); wait_v_bar<14>(); zstage(1); wait_v_bar<12>(); zstage(2);
        } else {
            const int tile = 16 * (xcd >> 1) + 8 * (xcd & 1) + (idx - 24);
            const char* xs = (const char*)(x + (size_t)(tile * 32) * D);
#pragma unroll
            for (int st = 0; st < 10; ++st) dma<0, 0, false>(xs + st * 128, zx, lq + st * 4096);
            for (int p = wave; p < 25; p += 4) { dma<40960, 0, false>((const char*)wk + p * 1024, lane * 16, l0 + p * 1024); dma<66560, 0, false>((const char*)wv + p * 1024, lane * 16, l0 + p * 1024); }
            if (blockIdx.x == 255 && wave == 0) { ST4(0u, WS_CNT + lane * 128); if (lane < 8) ST4(0u, WS_CNT + (64 + lane) * 128); }
            asm volatile("s_load_dwordx2 %0, %1, 0x0\n\ts_waitcnt lgkmcnt(0)" : "=s"(tag) : "s"(ws) : "memory");
            wait_v_bar<0>(); BAR(); BAR();
            if (hasg) { zstage(0); zstage(1); zstage(2); }
        }
        tag += 1;
        STAMP(1);
        auto publish1 = [&]() { if (wave == 0 && lane == 0) __hip_atomic_store((gu64*)(ws + (big ? WS_F1P + (p1 * 16 + blk1) * 8 : WS_F1K + (16 * (xcd >> 1) + 8 * (xcd & 1) + idx - 24) * 8)), tag, __ATOMIC_RELAXED, __HIP_MEMORY_SCOPE_AGENT); };
        auto kvpiece = [&](int g, int i) { dma<L_KV, 0, true>(kv + (size_t)g * KVG + i * 4096, wave * 1024 + lane * 16, lq + (g & 3) * 8192 + i * 4096); };
        auto wait_f1 = [&]() { if (wave == 0) poll_tags(ws + WS_F1K + lane * 8, ws + WS_F1P + ((tok0 >> 7) * 16 + min(lane, 11)) * 8, tag); };
        if (!big) publish1();
        if (hasg) {
#pragma unroll 1
            for (int n = 0; n < 7; ++n) { wait_v_bar<12>(); if (n == 2 && big) publish1(); zstage(n + 3); }
            STAMP(11);
            wait_v_bar<12>(); drain(); wait_f1(); BAR();
        } else {
            if (PF & 8) {
#pragma unroll 1
                for (int i = (idx - 28) * 4 + wave; i < 128; i += 16) touch((const char*)w_sp + (size_t)xcd * (256 * S * 2) + i * 8192, lane * 128, l0 + L_MISC + 64);
            }
            wait_f1(); BAR();
        }
        STAMP(12);
        {
            const char* zr = ws + WS_Z2R + (size_t)(tok0 >> 5) * (27 * 2048) + (tok0 & 31) * 16;
#pragma unroll
            for (int i = 0; i < 4; ++i) if (i < 3 || wave < 2) dma<L_LNR, 0, true>(zr + (wave + 4 * i) * 4096, r * 512 + (lane & 7) * 16, lq + i * 4096);
        }
        if (hasg) BAR();
#pragma unroll
        for (int g = 0; g < 4; ++g) { kvpiece(g, 0); kvpiece(g, 1); }
        STAMP(2);
        wait_v_bar<8>();
        layernorm();
        asm volatile("s_waitcnt vmcnt(%0) lgkmcnt(0)\n\ts_barrier" :: "i"(4) : "memory");
#pragma unroll
        for (int it = 0; it < 4; ++it) {
            const int cb = wave + 4 * it;
            if (cb < 14) {
                ldsp p = lds + L_LNT + ((lane & 15) >> 2) * (ZW * 2) + (cb * 64 + (lane >> 4) * 16 + (lane & 3) * 4) * 2;
                const s16x8 c = cat(ldtr(p), ldtr(p + 4 * (ZW * 2)));
                ST16(__builtin_bit_cast(u32x4, c), WS_Z2N + ((cb * 2 + h) * 128 + (tok0 >> 4)) * 1024 + ((tok0 >> 3) & 1) * 512 + r32 * 16);
            }
        }
        drain();
        if (lane == 0 && __hip_atomic_fetch_add((__attribute__((address_space(3))) unsigned*)(lds + L_MISC + 16), 1u, __ATOMIC_RELAXED, __HIP_MEMORY_SCOPE_WORKGROUP) == 3u)
            __hip_atomic_fetch_add((gu32*)(ws + WS_CNT + xcd * 128), 1u, __ATOMIC_RELAXED, __HIP_MEMORY_SCOPE_AGENT);
        STAMP(6);
        const unsigned gx = (wave * 8 + r) * (S * 2) + csw;
        auto gstage = [&](int n) {
            const unsigned lx = lq + L_X + (n & 3) * 8192, lw = lq + L_W + (n & 3) * 16384;
            const char* xp = (const char*)(w_sp + (size_t)panel * 64 * S) + n * 128; const char* wp = ws + WS_Z2N + (size_t)fb * 4 * 131072 + n * 4096;
            const unsigned wo_ = wave * 1024 + lane * 16;
            dma<0, 0, false>(xp, gx, lx); dma<4096, 0, false>(xp, gx + 32 * S * 2, lx);
            dma<0, 0, true>(wp, wo_, lw); dma<4096, 0, true>(wp + 131072, wo_, lw); dma<8192, 0, true>(wp + 2 * 131072, wo_, lw); dma<12288, 0, true>(wp + 3 * 131072, wo_, lw);
        };
        const unsigned ow = (wave * 8 + r) * (D * 2) + wsw;
        auto ostage_w = [&](int n) {
            const char* wp = n < 14 ? (const char*)(w_out + nb * 128) + (size_t)n * (64 * D * 2) : (const char*)(wo + nb * 128) + (size_t)(n - 14) * (64 * D * 2);
            wpieces(wp, ow, n == 13 ? ow : ow + 32 * D * 2, lq + L_W + (n & 3) * 16384);
        };
        auto ostage_x = [&](int n) {
            const char* xp = ws + WS_A + (size_t)po * 2 * NKS * 1024 + n * 4096;
            const unsigned lx = lq + L_X + (n & 3) * 8192, xo_ = wave * 1024 + lane * 16;
            dma<0, 0, true>(xp, xo_, lx); dma<4096, 0, true>(xp + NKS * 1024, xo_, lx);
        };
        auto add1 = [&](int c) { if (wave == 0 && lane == 0) __hip_atomic_fetch_add((gu32*)(ws + WS_CNT + c * 128), 1u, __ATOMIC_RELAXED, __HIP_MEMORY_SCOPE_AGENT); };
        for (int attempt = 0;; ++attempt) {
            const bool weave = attempt == 0 && hasg;
            const int gend = weave ? NAG : 21;
#pragma unroll 1
            for (int g = 1; g < gend; ++g) {
                if (g + 2 > 21) wait_v_bar<0>(); else wait_v_bar<2>();
                if (g + 3 <= 21) { kvpiece(g + 3, 0); kvpiece(g + 3, 1); }
                if ((PF & 16) && !hasg && g == PFG) {
#pragma unroll 1
                    for (int i = (idx - 28) * 4 + wave; i < 235; i += 16) touch(i < 135 ? (const char*)w_out + i * 8192 : (const char*)wo + (i - 135) * 8192, lane * 128, l0 + L_MISC + 64);
                }
            }
            if (weave) {
                poll_count(ws + WS_CNT + (lane & 7) * 128, 32u);
                if (blockIdx.x == 0 && wave == 0 && lane == 0)
                    __hip_atomic_store((gu64*)(ws + WS_EPOCH), tag, __ATOMIC_RELAXED, __HIP_MEMORY_SCOPE_AGENT);
                STAMP(3);
                gstage(0); gstage(1); gstage(2);
#pragma unroll 1
                for (int n = 0; n < 32; ++n) {
                    if (n < 30) wait_v_bar<12>(); else if (n == 30) wait_v_bar<8>(); else wait_v_bar<2>();
                    if (n + 3 < 32) gstage(n + 3);
                    kvpiece(NAG + 3 + (n >> 1), n & 1);
                }
                static_assert(NAG == 2, "the tail below is written for groups 18, 19, 20");
                wait_v_bar<2>();
                kvpiece(21, 0); kvpiece(21, 1);
                if (haso) { ostage_w(0); ostage_w(1); ostage_w(2); wait_v_bar<14>(); } else wait_v_bar<2>();
                add1(8 + panel);
                if (haso) wait_v_bar<12>(); else wait_v_bar<0>();
                if (haso) { poll_count(ws + WS_CNT + (8 + po) * 128, 7u);
                    if (PF & 32) { const int w_ = nb * 4 + wave; if (w_ < 14) touch(ws + WS_A + (size_t)(po * 2 + (w_ & 1)) * NKS * 1024 + (w_ >> 1) * 8192, lane * 128, l0 + L_LNT + wave * 256); }
                    ostage_x(0); ostage_x(1); ostage_x(2); }
                STAMP(4);
            }
            wait_l_bar();
            if (attempt == 1 || !__builtin_amdgcn_readfirstlane(badl[0] | badl[1] | badl[2] | badl[3])) break;
            BAR();
#pragma unroll
            for (int gg = 0; gg < 4; ++gg) { kvpiece(gg, 0); kvpiece(gg, 1); }
            wait_v_bar<0>();
        }
#pragma unroll
        for (int it = 0; it < 3; ++it) {
            const int id = it * 256 + wave * 64 + lane;
            if (it < 2 || wave < 2) ST16(*(const u32x4*)(smem + L_LNR + id * 16), WS_A + ((tok0 >> 5) * NKS + 56) * 1024 + (id >> 3) * 512 + ((tok0 & 31) + (id & 7)) * 16);
        }
        if (!haso) { wait_v_bar<0>(); add1(40 + (tok0 >> 6)); return; }
        wait_v_bar<4>(); ostage_x(3); ostage_w(3);
        wait_v_bar<6>(); add1(40 + (tok0 >> 6)); ostage_x(4); ostage_w(4);
#pragma unroll 1
        for (int n = 2; n < 21; ++n) {
            wait_v_bar<12>();
            if (n + 3 == 14) { poll_count(ws + WS_CNT + (40 + po) * 128, 8u); STAMP(5);
                if (PF & 32) { const int w_ = nb * 4 + wave; if (w_ < 10) touch(ws + WS_A + (size_t)((po * 2 + (w_ & 1)) * NKS + 56) * 1024 + (w_ >> 1) * 8192, lane * 128, l0 + L_LNT + wave * 256); } }
            ostage_x(n + 3); ostage_w(n + 3);
        }
        wait_v_bar<12>(); wait_v_bar<6>(); wait_v_bar<0>();
        return;
    }

    const unsigned lscr = l0 + L_LNT + wave * 256;
    const int wid = idx * 4 + wave;
    if (PF & 1) {
        if (wid < 5) touch((const char*)(uintptr_t)(pc0 & ~127ull) + wid * 8192, lane * 128, lscr);
    }
    if (PF & 2) {
        if (wid < 80) touch(x + (size_t)(xcd >> 1) * 512 * D, (wid * 64 + lane) * 128, lscr);
        if (wid < 120) { const int b6 = wid / 20, ln = (wid % 20) * 64 + lane, rw_ = ln >> 1, bb = 2 * b6 + (xcd & 1);
            const u16* base = bb < 5 ? wq : w_in;
            const unsigned off = bb < 5 ? rw_ * (D * 2) + bb * 256 + (ln & 1) * 128 : rw_ * (F2 * 2) + (FH - 32 + (bb - 5) * 128) * 2 + (ln & 1) * 128;
            touch(base, off, lscr); }
    }
    if ((PF & 4) && !hasg) {
        for (int i = (idx - 28) * 4 + wave; i < 140; i += 16) { const int ln = i * 64 + lane; touch(w_in, (ln / 14) * (F2 * 2) + (ln % 14) * 128, lscr); }
    }
    if (big) {
        f32x16 acc[2][2] = {};
        gemm_compute<2, 0, 10>(acc, lds, wave, lane, [] {});
        STAMP(1);
        const float sc = isq ? 0.22360679775f * 1.44269504089f : 1.0f;
#pragma unroll
        for (int tt = 0; tt < 2; ++tt) {
            const int tb = p1 * 4 + wt * 2 + tt;
#pragma unroll
            for (int fh = 0; fh < 2; ++fh) {
                const int c = col1 + wf * 64 + fh * 32;
                if (isq || c >= FH) {
                    const unsigned o = (isq ? WS_Q + (tb * 20 + (c >> 5)) * 2048 : WS_Z2R + (tb * 27 + ((c - FH) >> 5)) * 2048) + lane * 16;
#pragma unroll
                    for (int s = 0; s < 2; ++s) {
                        const f32x16& a = acc[tt][fh];
                        ST16(((u32x4){pk2(a[8 * s] * sc, a[8 * s + 1] * sc), pk2(a[8 * s + 2] * sc, a[8 * s + 3] * sc),
                                      pk2(a[8 * s + 4] * sc, a[8 * s + 5] * sc), pk2(a[8 * s + 6] * sc, a[8 * s + 7] * sc)}), o + s * 1024);
                    }
                }
            }
        }
    } else {
        const int tile = 16 * (xcd >> 1) + 8 * (xcd & 1) + (idx - 24);
        constexpr int LWK = 40960, LWV = LWK + 25600;
        u32x4 gw[10];
#pragma unroll
        for (int i = 0; i < 10; ++i) gw[i] = *(const u32x4*)(gate_w + (wave * 10 + i) * 16 + 8 * h);
        BAR();
        f32x16 ak = {}, av = {};
        float gs = 0.f;
        const int blk = (lane >> 4) & 1, q = (lane & 15) >> 2, pp = lane & 3;
        const int quad = min(blk * 4 + pp, 4);
#pragma unroll
        for (int i = 0; i < 10; ++i) {
            const int ks = wave * 10 + i, st = ks >> 2, kk = ks & 3;
            const s16x8 xf = ld128(lds + st * 4096 + r32 * 128 + (((2 * kk + h) ^ ((r32 >> 1) & 7)) << 4));
            const int row = ks * 16 + 8 * h + q;
            const s16x8 kf = cat(ldtr(lds + LWK + row * 40 + quad * 8), ldtr(lds + LWK + (row + 4) * 40 + quad * 8));
            const s16x8 vf = cat(ldtr(lds + LWV + row * 40 + quad * 8), ldtr(lds + LWV + (row + 4) * 40 + quad * 8));
            ak = mfma16(kf, xf, ak);
            av = mfma16(xf, vf, av);
            const u32x4 xu = __builtin_bit_cast(u32x4, xf);
#pragma unroll
            for (int j = 0; j < 4; ++j) gs += bflo(xu[j]) * bflo(gw[i][j]) + bfhi(xu[j]) * bfhi(gw[i][j]);
        }
        float* red = (float*)(smem + L_KV);
#pragma unroll
        for (int r = 0; r < 16; ++r) { red[(wave * 33 + r) * 64 + lane] = ak[r]; red[(wave * 33 + 16 + r) * 64 + lane] = av[r]; }
        red[(wave * 33 + 32) * 64 + lane] = gs;
        wait_l_bar();
        const unsigned img = WS_KV + tile * KVT;
        auto rsum = [&](int r) { return red[r * 64 + lane] + red[(33 + r) * 64 + lane] + red[(66 + r) * 64 + lane] + red[(99 + r) * 64 + lane]; };
        if (wave == 0) {
            float v[12];
#pragma unroll
            for (int r = 0; r < 12; ++r) v[r] = rsum(r);
            ST16(((u32x4){pk2(v[0], v[1]), pk2(v[2], v[3]), pk2(v[4], v[5]), pk2(v[6], v[7])}), img + h * 512 + r32 * 16);
            if (h == 0) { ST4(pk2(v[8], v[9]), img + 2304 + r32 * 4); ST4(pk2(v[10], v[11]), img + 2304 + 128 + r32 * 4); }
        } else if (wave < 3) {
            const int s = wave - 1;
            float v[8];
#pragma unroll
            for (int r = 0; r < 8; ++r) v[r] = rsum(16 + 8 * s + r);
            if (r32 < 20) ST16(((u32x4){pk2(v[0], v[1]), pk2(v[2], v[3]), pk2(v[4], v[5]), pk2(v[6], v[7])}), img + 1024 + s * 640 + h * 320 + r32 * 16);
        } else {
            float g = rsum(32);
            auto rr = __builtin_amdgcn_permlane32_swap(__float_as_uint(g), __float_as_uint(g), false, false);
            g = __uint_as_float(rr[0]) + __uint_as_float(rr[1]);
            if (h == 0) ST4(__float_as_uint(1.0f / (1.0f + __expf(-g))), WS_GATE + (tile * 32 + r32) * 4);
            if (lane < 2) { const unsigned c = lane ? 0u : 0x3f803f80u; ST16(((u32x4){c, c, c, c}), img + 2560 + lane * 16); }
        }
        wait_all_bar();
    }
    STAMP(2);
    f32x16 acc[1][2] = {};
    u32x4 z1[2][2];
    if (hasg) gemm_compute<1, 0, 10>(acc, lds, wave, lane, [] { drain(); });
    else BAR();
    STAMP(3);
    s16x8 qa1, qb1; u32x2 qa2, qb2; float gate_a, gate_b, g1; unsigned braw;
    {
        const int ta = tok0 + wave * 2;
        auto qoff = [&](int qd) { return WS_Q + ((ta >> 5) * 20 + (qd >> 3)) * 2048 + ((qd >> 2) & 1) * 1024 + (qd & 1) * 512 + (ta & 31) * 16 + ((qd >> 1) & 1) * 8; };
        const unsigned o0 = qoff(5 * r32 + h), o1 = qoff(5 * r32 + 2 + h), o2 = qoff(5 * r32 + 4) + 4 * h;
        const u32x2 a0 = __builtin_amdgcn_raw_buffer_load_b64(wsr, o0, 0, 16), a1 = __builtin_amdgcn_raw_buffer_load_b64(wsr, o1, 0, 16);
        const u32x2 b0 = __builtin_amdgcn_raw_buffer_load_b64(wsr, o0 + 16, 0, 16), b1 = __builtin_amdgcn_raw_buffer_load_b64(wsr, o1 + 16, 0, 16);
        qa1 = __builtin_bit_cast(s16x8, (u32x4){a0[0], a0[1], a1[0], a1[1]});
        qb1 = __builtin_bit_cast(s16x8, (u32x4){b0[0], b0[1], b1[0], b1[1]});
        qa2 = (u32x2){__builtin_amdgcn_raw_buffer_load_b32(wsr, o2, 0, 16), 0u};
        qb2 = (u32x2){__builtin_amdgcn_raw_buffer_load_b32(wsr, o2 + 16, 0, 16), 0u};
        gate_a = __uint_as_float(__builtin_amdgcn_raw_buffer_load_b32(wsr, WS_GATE + ta * 4, 0, 16));
        gate_b = __uint_as_float(__builtin_amdgcn_raw_buffer_load_b32(wsr, WS_GATE + ta * 4 + 4, 0, 16));
        const int t = (hasg ? panel : 0) * 64 + wt * 32 + r32;
        braw = b_sp[t]; g1 = __uint_as_float(__builtin_amdgcn_raw_buffer_load_b32(wsr, WS_GATE + t * 4, 0, 16));
        SBAR();
    }
    if (hasg) {
#pragma unroll
        for (int fh = 0; fh < 2; ++fh) {
#pragma unroll
            for (int j = 0; j < 8; ++j) z1[fh][j >> 2][j & 3] = pk2(gelu_t(acc[0][fh][2 * j]), gelu_t(acc[0][fh][2 * j + 1]));
            acc[0][fh] = (f32x16){};
        }
    }
    STAMP(4);
    BAR();
    STAMP(13);
    layernorm();
    wait_l_bar();
    STAMP(5);
    PIN(qa1); PIN(qb1); PIN(qa2); PIN(qb2); PIN(gate_a); PIN(gate_b); PIN(braw); PIN(g1);
    const float bias = bflo(braw); g1 = 1.0f - g1;
    const unsigned kone = h == 0 ? 0x3f803f80u : 0u;
    const f32x16 zero = {};
    ldsp xb[4], wb;
    { const int row = wt * 32 + r32; const unsigned a0 = row * 128 + ((h ^ ((row >> 1) & 7)) << 4);
#pragma unroll
      for (int kk = 0; kk < 4; ++kk) xb[kk] = lds + L_X + (a0 ^ (kk << 5));
      wb = lds + L_W + wf * 8192 + lane * 16; }
    for (int attempt = 0;; ++attempt) {
        const int oc = (r32 == 20 || r32 == 24) ? 2560 : 2576;
        ldsp kp = lds + L_KV + h * 512 + r32 * 16, k2p = lds + L_KV + 2304 + h * 128 + r32 * 4;
        ldsp vp0 = lds + L_KV + (r32 < 20 ? 1024 + h * 320 + r32 * 16 : oc), vp1 = vp0 + (r32 < 20 ? 640 : 0);
        s16x8 k1 = ld128(kp); u32x2 k2 = {ld32(k2p), kone};
        s16x8 v0 = ld128(vp0), v1 = v0, v0b;
        if (attempt == 0) {
            qa2[1] = neg_m_slots(xhalf_max(max16(mfma8(k2, qa2, mfma16(k1, qa1, zero)))), h);
            qb2[1] = neg_m_slots(xhalf_max(max16(mfma8(k2, qb2, mfma16(k1, qb1, zero)))), h);
        }
        f32x16 oa = zero, ob = zero;
        f32x16 sa = mfma8(k2, qa2, mfma16(k1, qa1, zero)), sb;
        u32x4 p0, p1 = {0u, 0u, 0u, 0u};
        s16x8 xf[4], wf0[4], wf1[4];
        xf[2] = (s16x8){}; xf[3] = (s16x8){}; wf1[2] = (s16x8){}; wf0[3] = (s16x8){}; wf1[3] = (s16x8){};
        STAMP(6);
#define B8(v) __builtin_bit_cast(s16x8, v)
#define EX(X, i) X[i] = __builtin_amdgcn_exp2f(X[i])
#define LDK(off) do { k1 = ld128(kp + (off)); k2[0] = ld32(k2p + (off)); } while (0)
#define LDV0(off) v0 = ld128(vp0 + (off))
#define LDV1(off) v1 = ld128(vp1 + (off))
#define C0(i, CUR) p0[i] = pk2(CUR[2 * (i)], CUR[2 * (i) + 1])
#define C1(i, CUR) p1[i] = pk2(CUR[8 + 2 * (i)], CUR[9 + 2 * (i)])
#define GAP(MF, RD, CUR, BODY) MF; SBAR(); RD; SBAR(); BODY; PIN(CUR); SBAR();
#define U4(CUR, NXT, QN1, QN2, O, PVO, R1, R2, R3)                                                                              \
        GAP(NXT = mfma16(k1, QN1, zero), NOP, CUR, { EX(CUR, 0); EX(CUR, 1); EX(CUR, 2); EX(CUR, 3); })                         \
        GAP(NXT = mfma8(k2, QN2, NXT), R1, CUR, { EX(CUR, 4); EX(CUR, 5); EX(CUR, 6); EX(CUR, 7); C0(0, CUR); C0(1, CUR); PIN(p0); }) \
        GAP(PVO = mfma16(v1, B8(p1), PVO), R2, CUR, { C0(2, CUR); C0(3, CUR); PIN(p0); SBAR(); EX(CUR, 8); EX(CUR, 9); EX(CUR, 10); EX(CUR, 11); }) \
        GAP(O = mfma16(v0, B8(p0), O), R3, CUR, { EX(CUR, 12); EX(CUR, 13); EX(CUR, 14); EX(CUR, 15); C1(0, CUR); C1(1, CUR); C1(2, CUR); C1(3, CUR); PIN(p1); })
#define U7(CUR, NXT, QN1, QN2, O, PVO, V0, G0, G1, G2, R0, R1, R2, R3, R4)                                                      \
        GAP(NXT = mfma16(k1, QN1, zero), R0, CUR, { EX(CUR, 0); EX(CUR, 1); })                                                  \
        GAP(G0, R1, CUR, { EX(CUR, 2); EX(CUR, 3); })                                                                           \
        GAP(NXT = mfma8(k2, QN2, NXT), R2, CUR, { EX(CUR, 4); EX(CUR, 5); C0(0, CUR); PIN(p0); })                               \
        GAP(G1, R3, CUR, { EX(CUR, 6); EX(CUR, 7); C0(1, CUR); C0(2, CUR); PIN(p0); })                                          \
        GAP(PVO = mfma16(v1, B8(p1), PVO), R4, CUR, { EX(CUR, 8); EX(CUR, 9); C0(3, CUR); PIN(p0); })                           \
        GAP(G2, NOP, CUR, { EX(CUR, 10); EX(CUR, 11); EX(CUR, 12); C1(0, CUR); PIN(p1); })                                      \
        GAP(O = mfma16(V0, B8(p0), O), NOP, CUR, { EX(CUR, 13); EX(CUR, 14); EX(CUR, 15); C1(1, CUR); C1(2, CUR); C1(3, CUR); PIN(p1); })
#define U6(CUR, NXT, QN1, QN2, O, PVO, V0, G0, G1, R0, R1, R2, R3)                                                              \
        GAP(NXT = mfma16(k1, QN1, zero), R0, CUR, { EX(CUR, 0); EX(CUR, 1); EX(CUR, 2); })                                      \
        GAP(G0, R1, CUR, { EX(CUR, 3); EX(CUR, 4); C0(0, CUR); PIN(p0); })                                                      \
        GAP(NXT = mfma8(k2, QN2, NXT), R2, CUR, { EX(CUR, 5); EX(CUR, 6); EX(CUR, 7); C0(1, CUR); PIN(p0); })                   \
        GAP(PVO = mfma16(v1, B8(p1), PVO), R3, CUR, { EX(CUR, 8); EX(CUR, 9); C0(2, CUR); C0(3, CUR); PIN(p0); })               \
        GAP(G1, NOP, CUR, { EX(CUR, 10); EX(CUR, 11); EX(CUR, 12); C1(0, CUR); PIN(p1); })                                      \
        GAP(O = mfma16(V0, B8(p0), O), NOP, CUR, { EX(CUR, 13); EX(CUR, 14); EX(CUR, 15); C1(1, CUR); C1(2, CUR); C1(3, CUR); PIN(p1); })
#define GM(kk, f) acc[0][f] = mfma16(wf##f[kk], xf[kk], acc[0][f])
#define RX(kk, S_) xf[kk] = ld128(xb[kk] + (S_) * 8192)
#define RW(kk, f, S_) wf##f[kk] = ld128(wb + (S_) * 16384 + (f) * 4096 + (kk) * 1024)
#define A_ sa, sb, qb1, qb2, oa, ob
#define B_ sb, sa, qa1, qa2, ob, oa
#define XP(m, ...) m(__VA_ARGS__)
#define TILE(T, KN, VN) XP(U4, A_, KN, LDV1(T), NOP) XP(U4, B_, NOP, NOP, VN)
#define BODY(T, S0, S1, VA, VB, KN, VN)                                                                                          \
        USE(VA); BAR(); SBAR();                                                                                                  \
        XP(U7, A_, VA, GM(2, 1), GM(3, 0), GM(3, 1), { RX(0, S0); RW(0, 0, S0); }, { RW(0, 1, S0); RX(1, S0); }, LDK((T) + KVT), NOP, LDV1(T)) \
        USE(v1); SBAR();                                                                                                         \
        XP(U6, B_, VA, GM(0, 0), GM(0, 1), { RW(1, 0, S0); RW(1, 1, S0); }, { RX(2, S0); RW(2, 0, S0); }, VB = ld128(vp0 + (T) + KVT), NOP) \
        USE(VB); SBAR();                                                                                                         \
        XP(U7, A_, VB, GM(1, 0), GM(1, 1), GM(2, 0), { RW(2, 1, S0); RX(3, S0); }, { RW(3, 0, S0); RW(3, 1, S0); }, LDK((T) + 2 * KVT), NOP, LDV1((T) + KVT)) \
        USE(v1); BAR(); SBAR();                                                                                                  \
        XP(U7, B_, VB, GM(2, 1), GM(3, 0), GM(3, 1), { RX(0, S1); RW(0, 0, S1); }, { RW(0, 1, S1); RX(1, S1); }, VA = ld128(vp0 + (T) + 2 * KVT), NOP, NOP) \
        USE(VA); SBAR();                                                                                                         \
        XP(U6, A_, VA, GM(0, 0), GM(0, 1), { RW(1, 0, S1); RW(1, 1, S1); }, { RX(2, S1); RW(2, 0, S1); }, KN, LDV1((T) + 2 * KVT)) \
        USE(v1); SBAR();                                                                                                         \
        XP(U7, B_, VA, GM(1, 0), GM(1, 1), GM(2, 0), { RW(2, 1, S1); RX(3, S1); }, { RW(3, 0, S1); RW(3, 1, S1); }, VN, NOP, NOP)
        int g = 0;
        for (int part = 0; part < 2; ++part) {
            const bool weave = attempt == 0 && part == 0 && hasg;
            const int gend = weave ? NAG : 21;
#pragma unroll 1
            for (; g < gend; ++g) {
                const int dn = (g & 3) == 3 ? -3 * 8192 : 8192;
                if (g) wait_l1_bar();
                TILE(0, LDK(KVT), LDV0(KVT)) TILE(KVT, LDK(2 * KVT), LDV0(2 * KVT))
                drain(); SBAR();
                TILE(2 * KVT, { kp += dn; k2p += dn; LDK(0); }, { vp0 += dn; vp1 += dn; LDV0(0); })
            }
            if (!weave) break;
            STAMP(7);
#pragma unroll 1
            for (int i = 0; i < 8; ++i) {
                const int dn = ((g >> 1) & 1) ? -16384 : 16384;
                BODY(0, 0, 1, v0, v0b, LDK(8192), v0b = ld128(vp0 + 8192))
                BODY(8192, 2, 3, v0b, v0, { kp += dn; k2p += dn; LDK(0); }, { vp0 += dn; vp1 += dn; LDV0(0); })
                g += 2;
            }
            GM(2, 1); GM(3, 0); GM(3, 1);
            STAMP(8);
#pragma unroll
            for (int fh = 0; fh < 2; ++fh) {
                const int ft = fb * 4 + wf * 2 + fh;
#pragma unroll
                for (int s = 0; s < 2; ++s) {
                    u32x4 o;
#pragma unroll
                    for (int j = 0; j < 4; ++j)
                        o[j] = pk2(g1 * bflo(z1[fh][s][j]) * (acc[0][fh][8 * s + 2 * j] + bias), g1 * bfhi(z1[fh][s][j]) * (acc[0][fh][8 * s + 2 * j + 1] + bias));
                    if (ft == 27) o = (u32x4){0u, 0u, 0u, 0u};
                    ST16(o, WS_A + ((panel * 2 + wt) * NKS + ft * 2 + s) * 1024 + lane * 16);
                }
            }
        }
        TILE(0, NOP, NOP)
        ob = mfma16(v1, B8(p1), ob);
        STAMP(9);
        const float la = h ? oa[8] : oa[12], lb = h ? ob[8] : ob[12];
        float mx = __builtin_fmaxf(la, lb) * 0x1p26f;
#pragma unroll
        for (int r = 0; r < 12; ++r) mx = __builtin_fmaxf(mx, __builtin_fmaxf(__builtin_fabsf(oa[r]), __builtin_fabsf(ob[r])));
        const bool bad = __any(!(mx < 0x1p126f));
        if (lane == 0) badl[wave] = bad;
        const float ia = gate_a * __builtin_amdgcn_rcpf(la), ib = gate_b * __builtin_amdgcn_rcpf(lb);
#pragma unroll
        for (int gq = 0; gq < 3; ++gq) {
            if (gq < 2 || h == 0) {
                const int qd = 5 * r32 + 2 * gq + h;
                char* dst = smem + L_LNR + (2 * (qd >> 2) + (qd & 1)) * 128 + wave * 32 + ((qd >> 1) & 1) * 8;
                *(u32x2*)dst = (u32x2){pk2(oa[4 * gq] * ia, oa[4 * gq + 1] * ia), pk2(oa[4 * gq + 2] * ia, oa[4 * gq + 3] * ia)};
                *(u32x2*)(dst + 16) = (u32x2){pk2(ob[4 * gq] * ib, ob[4 * gq + 1] * ib), pk2(ob[4 * gq + 2] * ib, ob[4 * gq + 3] * ib)};
            }
        }
        wait_l_bar();
        if (attempt == 1 || !__builtin_amdgcn_readfirstlane(badl[0] | badl[1] | badl[2] | badl[3])) break;
        BAR();
        qa2[1] = 0u; qb2[1] = 0u;
        float ma = -__builtin_inff(), mb = -__builtin_inff();
#pragma unroll 1
        for (int j = 0; j < 64; ++j) {
            const s16x8 c1 = B8(__builtin_amdgcn_raw_buffer_load_b128(wsr, WS_KV + j * KVT + h * 512 + r32 * 16, 0, 16));
            const u32x2 c2 = {__builtin_amdgcn_raw_buffer_load_b32(wsr, WS_KV + j * KVT + 2304 + h * 128 + r32 * 4, 0, 16), kone};
            ma = __builtin_fmaxf(ma, max16(mfma8(c2, qa2, mfma16(c1, qa1, zero))));
            mb = __builtin_fmaxf(mb, max16(mfma8(c2, qb2, mfma16(c1, qb1, zero))));
        }
        qa2[1] = neg_m_slots(xhalf_max(ma), h);
        qb2[1] = neg_m_slots(xhalf_max(mb), h);
        BAR();
    }
    STAMP(10);
    if (!haso) { BAR(); return; }
    acc[0][0] = zero; acc[0][1] = zero;
    gemm_compute<1, 1, 24>(acc, lds, wave, lane, [] {});
    STAMP(11);
    const int t = po * 64 + wt * 32 + r32;
#pragma unroll
    for (int fh = 0; fh < 2; ++fh) {
        char* dst = (char*)(out + (size_t)t * D + nb * 128 + wf * 64 + fh * 32) + 16 * h;
#pragma unroll
        for (int q = 0; q < 2; ++q) {
            const f32x16& a = acc[0][fh];
            auto s0 = __builtin_amdgcn_permlane32_swap(pk2(a[8 * q], a[8 * q + 1]), pk2(a[8 * q + 4], a[8 * q + 5]), false, false);
            auto s1 = __builtin_amdgcn_permlane32_swap(pk2(a[8 * q + 2], a[8 * q + 3]), pk2(a[8 * q + 6], a[8 * q + 7]), false, false);
            *(u32x4*)(dst + 32 * q) = (u32x4){s0[0], s1[0], s0[1], s1[1]};
        }
    }
    STAMP(12);
}

extern "C" void kernel_launch(void* const* d_in, const int* in_sizes, int n_in, void* d_out, int out_size, void* d_ws, size_t ws_size,
                              hipStream_t stream) {
    (void)hipFuncSetAttribute((const void*)fused, hipFuncAttributeMaxDynamicSharedMemorySize, LDS_BYTES);
    fused<<<256, 512, LDS_BYTES, stream>>>((const u16*)d_in[0], (const u16*)d_in[1], (const u16*)d_in[2], (const u16*)d_in[3], (const u16*)d_in[4],
                                           (const u16*)d_in[6], (const u16*)d_in[7], (const u16*)d_in[8], (const u16*)d_in[9], (const u16*)d_in[10],
                                           (char*)d_ws, (u16*)d_out);
}
```
